```python
import torch

def _softmax(x, axis=-1):
    return torch.softmax(x, dim=axis)
def _gelu(x):
    return torch.nn.functional.gelu(x, approximate="tanh")
def _silu(x):
    return torch.nn.functional.silu(x)
def _sigmoid(x):
    return torch.sigmoid(x)
def _layernorm(x, axis=-1, eps=1e-5):
    m = x.mean(dim=axis, keepdim=True)
    v = x.var(dim=axis, keepdim=True, unbiased=False)
    return (x - m) / torch.sqrt(v + eps)
def _scatter_add(tgt, idx, src, axis=0):
    return tgt.clone().index_add_(axis, idx, src)
def _segment_sum(src, seg, n):
    out = torch.zeros((n,) + src.shape[1:], dtype=src.dtype)
    return out.index_add_(0, seg, src)
def _pad(x, pw):
    flat = tuple(v for pair in reversed(pw) for v in pair)
    return torch.nn.functional.pad(x, flat)

kernel_name = 'sequential_preLN__compressed_kv_cla__glu_gated_reglu__decode'

def setup_inputs():

    _w = (lambda a: (lambda t: t.to(torch.bfloat16) if t.is_floating_point() else t)(torch.as_tensor(a)))
    def _A_setup():
        B, H, Sq, Sk, D, M = 1, 32, 1, 4096, 128, 640
        Dc = 4 * D
        return {
            "x": _w((lambda *s: torch.randn(*s, dtype=torch.bfloat16) * 0.05)(B, Sq, M)),
            "c_kv": _w((lambda *s: torch.randn(*s, dtype=torch.bfloat16) * 0.8)(B, Sk, Dc)),
            "w_q": _w((lambda *s: torch.randn(*s, dtype=torch.bfloat16) * 1.6)(M, H, D) * (M ** -0.5)),
            "w_uk": _w((lambda *s: torch.randn(*s, dtype=torch.bfloat16) * 1.6)(Dc, H, D) * (Dc ** -0.5)),
            "w_uv": _w((lambda *s: torch.randn(*s, dtype=torch.bfloat16) * 0.8)(Dc, H, D) * (Dc ** -0.5)),
            "w_o": _w((lambda *s: torch.randn(*s, dtype=torch.bfloat16) * 0.8)(H, D, M) * ((H * D) ** -0.5)),
        }
    _A_in = _A_setup()

    def _B_setup():
        B, S, D, F = 1, 1, 640, 1728
        return {
            "x": _w((lambda *s: torch.randn(*s, dtype=torch.bfloat16) * 0.05)(B, S, D)),
            "W_gate": _w((lambda *s: torch.randn(*s, dtype=torch.bfloat16) * 0.4)(D, F) * (2.0 / D) ** 0.5),
            "W_up": _w((lambda *s: torch.randn(*s, dtype=torch.bfloat16) * 0.4)(D, F) * (2.0 / D) ** 0.5),
            "W_down": _w((lambda *s: torch.randn(*s, dtype=torch.bfloat16) * 0.4)(F, D) * (1.0 / F) ** 0.5),
        }
    _B_in = _B_setup()

    return {
        "A_x": _A_in["x"],
        "A_c_kv": _A_in["c_kv"],
        "A_w_q": _A_in["w_q"],
        "A_w_uk": _A_in["w_uk"],
        "A_w_uv": _A_in["w_uv"],
        "A_w_o": _A_in["w_o"],
        "B_x": _B_in["x"],
        "B_W_gate": _B_in["W_gate"],
        "B_W_up": _B_in["W_up"],
        "B_W_down": _B_in["W_down"],
    }

def reference(A_x, A_c_kv, A_w_q, A_w_uk, A_w_uv, A_w_o, B_x, B_W_gate, B_W_up, B_W_down):
    def _A_ref(x, c_kv, w_q, w_uk, w_uv, w_o):
        B, Sq, M = x.shape[0], x.shape[1], x.shape[2]
        Sk = c_kv.shape[1]
        H, D = w_q.shape[1], w_q.shape[2]
        q = torch.einsum("bqm,mhd->bhqd", x, w_q)
        k = torch.einsum("bkc,chd->bhkd", c_kv, w_uk)
        v = torch.einsum("bkc,chd->bhkd", c_kv, w_uv)
        scores = torch.einsum("bhqd,bhkd->bhqk", q, k) * (D ** -0.5)
        off = Sk - Sq
        qi = torch.arange(Sq)[:, None]
        kj = torch.arange(Sk)[None, :]
        cmask = kj <= (qi + off)
        scores = torch.where(cmask[None, None], scores, -1e9)
        p = _softmax(scores, axis=-1)
        ctx = torch.einsum("bhqk,bhkd->bhqd", p, v)
        return torch.einsum("bhqd,hdm->bqm", ctx, w_o)

    def _B_ref(x, W_gate, W_up, W_down):
        gate = torch.einsum("bsd,df->bsf", x, W_gate)
        up = torch.einsum("bsd,df->bsf", x, W_up)
        gate = gate * (lambda x, dt: x.to(dt))(gate > 0, gate.dtype)
        h = gate * up
        return torch.einsum("bsf,fd->bsd", h, W_down)

    x = A_x
    h = x + _A_ref(_layernorm(x, axis=-1), A_c_kv, A_w_q, A_w_uk, A_w_uv, A_w_o)
    h2 = h + _B_ref(_layernorm(h, axis=-1), B_W_gate, B_W_up, B_W_down)
    return h2
```

```cpp
#include <hip/hip_runtime.h>
#include <hip/hip_bf16.h>
typedef unsigned short u16;
__device__ __forceinline__ float bf2f(u16 b) { return __uint_as_float(((unsigned)b) << 16); }
__device__ __forceinline__ u16 f2bf(float f) { unsigned u = __float_as_uint(f); u += 0x7FFFu + ((u >> 16) & 1u); return (u16)(u >> 16); }
constexpr int M = 640, H = 32, D = 128, C = 512, SK = 4096, F = 1728;

__device__ void ln_to_lds(const float* v, float* out, float* red) {
  int t = threadIdx.x;
  float s = 0.f;
  for (int i = t; i < M; i += blockDim.x) s += v[i];
  for (int o = 32; o > 0; o >>= 1) s += __shfl_xor(s, o);
  if ((t & 63) == 0) red[t >> 6] = s;
  __syncthreads();
  float tot = 0.f;
  for (int i = 0; i < (int)(blockDim.x >> 6); ++i) tot += red[i];
  float mean = tot / M;
  __syncthreads();
  float q = 0.f;
  for (int i = t; i < M; i += blockDim.x) { float d = v[i] - mean; q += d * d; }
  for (int o = 32; o > 0; o >>= 1) q += __shfl_xor(q, o);
  if ((t & 63) == 0) red[t >> 6] = q;
  __syncthreads();
  float tq = 0.f;
  for (int i = 0; i < (int)(blockDim.x >> 6); ++i) tq += red[i];
  float rs = 1.0f / sqrtf(tq / M + 1e-5f);
  for (int i = t; i < M; i += blockDim.x) out[i] = (v[i] - mean) * rs;
  __syncthreads();
}

__global__ void k_q(const u16* x, const u16* wq, float* q) {
  __shared__ float xv[M], xn[M], red[16];
  for (int i = threadIdx.x; i < M; i += blockDim.x) xv[i] = bf2f(x[i]);
  __syncthreads();
  ln_to_lds(xv, xn, red);
  int n = blockIdx.x * blockDim.x + threadIdx.x;
  float acc = 0.f;
  for (int k = 0; k < M; ++k) acc += xn[k] * bf2f(wq[(size_t)k * (H * D) + n]);
  q[n] = acc;
}
__global__ void k_qabs(const float* q, const u16* wuk, float* qa) {
  int idx = blockIdx.x * blockDim.x + threadIdx.x;
  int h = idx / C, c = idx % C;
  float acc = 0.f;
  const u16* w = wuk + ((size_t)c * H + h) * D;
  for (int d = 0; d < D; ++d) acc += q[h * D + d] * bf2f(w[d]);
  qa[idx] = acc * 0.08838834764831845f;
}
__global__ void k_scores(const float* qa, const u16* ckv, float* sc) {
  int idx = blockIdx.x * blockDim.x + threadIdx.x;
  int h = idx / SK, k = idx % SK;
  float acc = 0.f;
  const u16* cr = ckv + (size_t)k * C;
  for (int c = 0; c < C; ++c) acc += qa[h * C + c] * bf2f(cr[c]);
  sc[idx] = acc;
}
__global__ void k_softmax(float* sc) {
  __shared__ float red[16];
  float* s = sc + (size_t)blockIdx.x * SK;
  int t = threadIdx.x;
  float m = -INFINITY;
  for (int i = t; i < SK; i += blockDim.x) m = fmaxf(m, s[i]);
  for (int o = 32; o > 0; o >>= 1) m = fmaxf(m, __shfl_xor(m, o));
  if ((t & 63) == 0) red[t >> 6] = m;
  __syncthreads();
  for (int i = 0; i < (int)(blockDim.x >> 6); ++i) m = fmaxf(m, red[i]);
  __syncthreads();
  float l = 0.f;
  for (int i = t; i < SK; i += blockDim.x) { float e = __expf(s[i] - m); s[i] = e; l += e; }
  for (int o = 32; o > 0; o >>= 1) l += __shfl_xor(l, o);
  if ((t & 63) == 0) red[t >> 6] = l;
  __syncthreads();
  float tot = 0.f;
  for (int i = 0; i < (int)(blockDim.x >> 6); ++i) tot += red[i];
  float inv = 1.0f / tot;
  for (int i = t; i < SK; i += blockDim.x) s[i] *= inv;
}
__global__ void k_ctxlat(const float* p, const u16* ckv, float* cl) {
  int idx = blockIdx.x * blockDim.x + threadIdx.x;
  int h = idx / C, c = idx % C;
  float acc = 0.f;
  for (int k = 0; k < SK; ++k) acc += p[h * SK + k] * bf2f(ckv[(size_t)k * C + c]);
  cl[idx] = acc;
}
__global__ void k_ctx(const float* cl, const u16* wuv, float* ctx) {
  int idx = blockIdx.x * blockDim.x + threadIdx.x;
  int h = idx / D, d = idx % D;
  float acc = 0.f;
  for (int c = 0; c < C; ++c) acc += cl[h * C + c] * bf2f(wuv[((size_t)c * H + h) * D + d]);
  ctx[idx] = acc;
}
__global__ void k_o(const u16* x, const float* ctx, const u16* wo, float* hbuf) {
  int m = blockIdx.x * blockDim.x + threadIdx.x;
  if (m >= M) return;
  float acc = 0.f;
  for (int k = 0; k < H * D; ++k) acc += ctx[k] * bf2f(wo[(size_t)k * M + m]);
  hbuf[m] = bf2f(x[m]) + acc;
}
__global__ void k_gu(const float* hbuf, const u16* wg, const u16* wu, float* hh) {
  __shared__ float xv[M], xn[M], red[16];
  for (int i = threadIdx.x; i < M; i += blockDim.x) xv[i] = hbuf[i];
  __syncthreads();
  ln_to_lds(xv, xn, red);
  int f = blockIdx.x * blockDim.x + threadIdx.x;
  if (f >= F) return;
  float g = 0.f, u = 0.f;
  for (int k = 0; k < M; ++k) { g += xn[k] * bf2f(wg[(size_t)k * F + f]); u += xn[k] * bf2f(wu[(size_t)k * F + f]); }
  hh[f] = (g > 0.f ? g : 0.f) * u;
}
__global__ void k_down(const float* hbuf, const float* hh, const u16* wd, u16* out) {
  int m = blockIdx.x * blockDim.x + threadIdx.x;
  if (m >= M) return;
  float acc = 0.f;
  for (int f = 0; f < F; ++f) acc += hh[f] * bf2f(wd[(size_t)f * M + m]);
  out[m] = f2bf(hbuf[m] + acc);
}
extern "C" void kernel_launch(void* const* d_in, const int* in_sizes, int n_in,
                              void* d_out, int out_size, void* d_ws, size_t ws_size,
                              hipStream_t stream) {
  const u16* x = (const u16*)d_in[0]; const u16* ckv = (const u16*)d_in[1];
  const u16* wq = (const u16*)d_in[2]; const u16* wuk = (const u16*)d_in[3];
  const u16* wuv = (const u16*)d_in[4]; const u16* wo = (const u16*)d_in[5];
  const u16* wg = (const u16*)d_in[7]; const u16* wu = (const u16*)d_in[8]; const u16* wd = (const u16*)d_in[9];
  float* ws = (float*)d_ws;
  float* q = ws; float* qa = q + 4096; float* sc = qa + 16384; float* cl = sc + 32 * 4096; float* ctx = cl + 16384;
  float* hb = ctx + 4096; float* hh = hb + 1024;
  k_q<<<64, 64, 0, stream>>>(x, wq, q);
  k_qabs<<<256, 64, 0, stream>>>(q, wuk, qa);
  k_scores<<<2048, 64, 0, stream>>>(qa, ckv, sc);
  k_softmax<<<32, 1024, 0, stream>>>(sc);
  k_ctxlat<<<256, 64, 0, stream>>>(sc, ckv, cl);
  k_ctx<<<64, 64, 0, stream>>>(cl, wuv, ctx);
  k_o<<<10, 64, 0, stream>>>(x, ctx, wo, hb);
  k_gu<<<27, 64, 0, stream>>>(hb, wg, wu, hh);
  k_down<<<10, 64, 0, stream>>>(hb, hh, wd, (u16*)d_out);
}
```

```cpp
#include <hip/hip_runtime.h>
#include <hip/hip_bf16.h>

typedef unsigned int u32;
typedef unsigned short u16;
typedef unsigned char u8;
typedef float f32x4 __attribute__((ext_vector_type(4)));
typedef float f32x2 __attribute__((ext_vector_type(2)));
typedef u32 u32x4 __attribute__((ext_vector_type(4)));
typedef u32 u32x2 __attribute__((ext_vector_type(2)));
typedef __bf16 bf16x8 __attribute__((ext_vector_type(8)));
typedef __bf16 bf16x2 __attribute__((ext_vector_type(2)));
typedef short s16x4 __attribute__((ext_vector_type(4)));
typedef __amdgpu_buffer_rsrc_t rsrc_t;

#define DEV __device__ __forceinline__

constexpr u32 WS_R2 = 4096;
constexpr u32 WS_FL = WS_R2 + 65536;
constexpr u32 WS_ML = WS_FL + 16384;
constexpr u32 WS_R6 = WS_ML + 16384;
constexpr u32 WS_R5 = WS_R6 + 4096;
constexpr u32 WS_R7 = WS_R5 + 20 * 128 * 128;
constexpr u32 WS_R3 = WS_R7 + 20 * 108 * 128 + 2048;
constexpr u32 WS_END = WS_R3 + 32 * 32 * 1024;

constexpr u32 L_WUK = 0, L_WUV = 32768, L_WO = 65536, L_QP = 106496, L_MLB = 114688, L_CTX = 114944;
constexpr u32 CKS = 1056;
constexpr u32 L_CKV = 0, L_QL = 128 * CKS, L_WM = L_QL + 8 * CKS, L_LP = L_WM + 256, L_PF = L_LP + 256;
constexpr u32 LDS_BYTES = L_PF + 4096;
constexpr u32 L_GATE = 0, L_UP = 20480, L_DOWN = 40960, L_ONES = 61440, L_HB = 62720, L_GP = 64000, L_ST = 65024;
constexpr u32 L_RED5 = 0, L_RED7 = 1024;

constexpr int SPIN_MAX = 1 << 14;
constexpr u32 SENT = 0xFFFFFFFFu;

DEV f32x4 mfma32(u32x4 a, u32x4 b, f32x4 c) {
  return __builtin_amdgcn_mfma_f32_16x16x32_bf16(__builtin_bit_cast(bf16x8, a), __builtin_bit_cast(bf16x8, b), c, 0, 0, 0);
}
DEV f32x4 mfma16(u32x2 a, u32x2 b, f32x4 c) {
  return __builtin_amdgcn_mfma_f32_16x16x16bf16_1k(__builtin_bit_cast(s16x4, a), __builtin_bit_cast(s16x4, b), c, 0, 0, 0);
}
DEV u32 cvtpk(float lo, float hi) {
  f32x2 v = {lo, hi};
  return __builtin_bit_cast(u32, __builtin_convertvector(v, bf16x2));
}
DEV float bflo(u32 u) { return __uint_as_float(u << 16); }
DEV float bfhi(u32 u) { return __uint_as_float(u & 0xffff0000u); }
template <int CTRL> DEV u32 dppu(u32 v) { return (u32)__builtin_amdgcn_update_dpp(0, (int)v, CTRL, 0xf, 0xf, true); }
template <int CTRL> DEV float dppf(float v) { return __uint_as_float(dppu<CTRL>(__float_as_uint(v))); }
constexpr int ROR8 = 0x128, ROR4 = 0x124, ROR2 = 0x122, ROR1 = 0x121, XOR1 = 0xB1, SHL4 = 0x104, NBC = 0x150;
DEV float red32(float a, float b) {
  auto s = __builtin_amdgcn_permlane32_swap(__float_as_uint(a), __float_as_uint(b), false, false);
  return __uint_as_float(s[0]) + __uint_as_float(s[1]);
}
DEV float red16(float a, float b) {
  auto s = __builtin_amdgcn_permlane16_swap(__float_as_uint(a), __float_as_uint(b), false, false);
  return __uint_as_float(s[0]) + __uint_as_float(s[1]);
}
DEV float max32(float a) {
  auto s = __builtin_amdgcn_permlane32_swap(__float_as_uint(a), __float_as_uint(a), false, false);
  return fmaxf(__uint_as_float(s[0]), __uint_as_float(s[1]));
}
DEV float max16(float a) {
  auto s = __builtin_amdgcn_permlane16_swap(__float_as_uint(a), __float_as_uint(a), false, false);
  return fmaxf(__uint_as_float(s[0]), __uint_as_float(s[1]));
}
DEV float wave_sum(float v) {
  v += dppf<ROR8>(v); v += dppf<ROR4>(v); v += dppf<ROR2>(v); v += dppf<ROR1>(v);
  v = red16(v, v);
  return red32(v, v);
}
DEV u32x2 trrd(const u8* p) {
  typedef short v4i16_t __attribute__((ext_vector_type(4)));
  return __builtin_bit_cast(u32x2, __builtin_amdgcn_ds_read_tr16_b64_v4i16((__attribute__((address_space(3))) v4i16_t*)p));
}
DEV u32x4 cat(u32x2 a, u32x2 b) { return u32x4{a.x, a.y, b.x, b.y}; }
DEV void dma16(const void* sbase, u32 voff, u32 lds) {
  u32 keep;
  asm volatile("s_mov_b32 %0, m0\n\ts_mov_b32 m0, %3\n\ts_nop 0\n\tglobal_load_lds_dwordx4 %1, %2\n\ts_mov_b32 m0, %0"
               : "=&s"(keep) : "v"(voff), "s"(sbase), "s"(lds) : "memory");
}
#define BAR() asm volatile("s_waitcnt lgkmcnt(0)\n\ts_barrier" ::: "memory")
#define WAITVM(n) asm volatile("s_waitcnt vmcnt(" #n ")" ::: "memory")
DEV u32x4 ld_dev(rsrc_t rs, u32 off) { return __builtin_amdgcn_raw_buffer_load_b128(rs, off, 0, 16); }
DEV void st_dev(rsrc_t rs, u32 off, u32x4 v) { __builtin_amdgcn_raw_buffer_store_b128(v, rs, off, 0, 16); }
DEV bool wave_any(bool p) { return __builtin_amdgcn_ballot_w64(p) != 0; }

template <int N> DEV void poll_tagged(rsrc_t rs, const u32 (&off)[N], const bool (&act)[N], u32 tag, u32x4 (&v)[N]) {
  for (int spin = 0; spin < SPIN_MAX; ++spin) {
    bool bad = false;
    asm volatile("" ::: "memory");
#pragma unroll
    for (int i = 0; i < N; ++i) {
      v[i] = u32x4{0, tag, 0, tag};
      if (act[i]) v[i] = ld_dev(rs, off[i]);
    }
#pragma unroll
    for (int i = 0; i < N; ++i) bad |= (v[i].y != tag) | (v[i].w != tag);
    if (!wave_any(bad)) break;
  }
}
DEV u32x4 granule(f32x4 a, u32 tag) { return u32x4{cvtpk(a[0], a[1]), tag, cvtpk(a[2], a[3]), tag}; }

template <int NP> DEV f32x4 reduce_lines(rsrc_t rs, u32 base, u32 tag, u8* red, int t, int w, int l) {
  u32 off[2]; bool act[2]; u32x4 v[2];
#pragma unroll
  for (int i = 0; i < 2; ++i) {
    int p = (t >> 3) + 64 * i;
    act[i] = p < NP;
    off[i] = base + p * 128 + (t & 7) * 16;
  }
  poll_tagged<2>(rs, off, act, tag, v);
  float a0 = bflo(v[0].x) + bflo(v[1].x), a1 = bfhi(v[0].x) + bfhi(v[1].x);
  float a2 = bflo(v[0].z) + bflo(v[1].z), a3 = bfhi(v[0].z) + bfhi(v[1].z);
  float r0 = red32(a0, a2), r1 = red32(a1, a3);
  float r = red16(r0, r1);
  r += dppf<ROR8>(r);
  if ((l & 8) == 0) *(float*)(red + w * 128 + (4 * (l & 7) + (l >> 4)) * 4) = r;
  BAR();
  f32x4 s = {0.f, 0.f, 0.f, 0.f};
  if (w == 0) {
#pragma unroll
    for (int i = 0; i < 8; ++i) s += *(const f32x4*)(red + i * 128 + (l & 7) * 16);
  }
  return s;
}

__global__ void __launch_bounds__(512, 2)
mla_ffn_decode_kernel(const u16* __restrict__ x, const u16* __restrict__ ckv, const u16* __restrict__ wq,
                      const u16* __restrict__ wuk, const u16* __restrict__ wuv, const u16* __restrict__ wo,
                      const u16* __restrict__ wg, const u16* __restrict__ wu, const u16* __restrict__ wd,
                      u16* __restrict__ out, u8* __restrict__ ws) {
  __shared__ __attribute__((aligned(16))) u8 smem[LDS_BYTES];
  const u32 lds0 = (u32)(size_t)(__attribute__((address_space(3))) u8*)smem;
  const int b = blockIdx.x, xc = b & 7, r = b >> 3, idx = r & 15;
  const int t = threadIdx.x, l = t & 63, q = l >> 4, j = l & 15;
  const int w = __builtin_amdgcn_readfirstlane(t >> 6);
  const rsrc_t rs = __builtin_amdgcn_make_buffer_rsrc((void*)ws, 0, WS_END, 0x00020000);
  const u32 epoch_v = __builtin_amdgcn_raw_buffer_load_b32(rs, 0, 0, 16);
  const f32x4 zero4 = {0.f, 0.f, 0.f, 0.f};

  if (r < 16) {
    const int h = 4 * xc + (idx >> 2), cq = idx & 3;
    if (w < 4) {
#pragma unroll
      for (int n = 0; n < 8; ++n) {
        int row = 32 * w + 4 * n + q;
        dma16(wuk, (u32)(((128 * cq + row) * 32 + h) * 256 + ((j ^ (row & 15)) * 16)), lds0 + L_WUK + (32 * w + 4 * n) * 256);
      }
    }
    const u32x4 xa = *(const u32x4*)((const u8*)x + l * 16);
    const u32 xb = *(const u32*)((const u8*)x + 1024 + l * 4);
    u32x2 xr[5];
#pragma unroll
    for (int k = 0; k < 5; ++k) xr[k] = *(const u32x2*)((const u8*)x + (k * 128 + (4 * w + q) * 4) * 2);
    u32x4 wv[20];
    {
      const u8* base = (const u8*)wq + h * 256;
      const u32 vo = (u32)((4 * w + q) * 4 * 8192 + j * 16);
#pragma unroll
      for (int i = 0; i < 20; ++i) wv[i] = *(const u32x4*)(base + ((i >> 2) * 128 + (i & 3)) * 8192 + vo);
    }
    __builtin_amdgcn_sched_barrier(0);
    WAITVM(27);
    u32x4 auk[2][4];
    if (w < 4) {
#pragma unroll
      for (int tt = 0; tt < 2; ++tt)
#pragma unroll
        for (int ks = 0; ks < 4; ++ks)
          auk[tt][ks] = *(const u32x4*)(smem + L_WUK + (32 * w + 16 * tt + j) * 256 + (((4 * ks + q) ^ j) * 16));
    }
    float mean, rstd;
    {
      float v[10] = {bflo(xa.x), bfhi(xa.x), bflo(xa.y), bfhi(xa.y), bflo(xa.z), bfhi(xa.z), bflo(xa.w), bfhi(xa.w), bflo(xb), bfhi(xb)};
      float s = 0.f;
#pragma unroll
      for (int i = 0; i < 10; ++i) s += v[i];
      mean = wave_sum(s) * (1.0f / 640.0f);
      float s2 = 0.f;
#pragma unroll
      for (int i = 0; i < 10; ++i) { float d = v[i] - mean; s2 += d * d; }
      rstd = 1.0f / sqrtf(wave_sum(s2) * (1.0f / 640.0f) + 1e-5f);
    }
    {
      u32 sel[4][2];
#pragma unroll
      for (int k = 0; k < 4; ++k) {
        bool own = ((j & 7) >> 1) == k;
        sel[k][0] = own ? ((j & 1) ? 0x01000c0cu : 0x0c0c0100u) : 0x0c0c0c0cu;
        sel[k][1] = own ? ((j & 1) ? 0x03020c0cu : 0x0c0c0302u) : 0x0c0c0c0cu;
      }
      f32x4 acc0 = zero4, acc1 = zero4;
#pragma unroll
      for (int i = 0; i < 20; ++i) {
        u32 xw = (i & 2) ? xr[i >> 2].y : xr[i >> 2].x;
        xw = (j < 8) ? xw : 0x3F803F80u;
        u32x4 bf;
#pragma unroll
        for (int k = 0; k < 4; ++k) bf[k] = __builtin_amdgcn_perm(xw, xw, sel[k][i & 1]);
        if (i & 1) acc1 = mfma32(wv[i], bf, acc1); else acc0 = mfma32(wv[i], bf, acc0);
      }
      acc0 += acc1;
#pragma unroll
      for (int e = 0; e < 4; ++e) {
        int P = 16 * q + 4 * e + ((j & 7) >> 1);
        int pos = P ^ ((q & 1) << 3);
        *(float*)(smem + L_QP + w * 1024 + pos * 16 + (j >> 3) * 8 + (j & 1) * 4) = acc0[e];
      }
    }
    BAR();
    const u32 tag = __builtin_amdgcn_readfirstlane(epoch_v) + 1u;
    if (w < 4) {
      const int P = 16 * (j >> 2) + 4 * q + (j & 3);
      const int pos = P ^ (((j >> 2) & 1) << 3);
      f32x4 s = zero4;
#pragma unroll
      for (int i = 0; i < 8; ++i) s += *(const f32x4*)(smem + L_QP + i * 1024 + pos * 16);
      const u32 pk = cvtpk(rstd * (s[0] - mean * s[2]), rstd * (s[1] - mean * s[3]));
      u32x4 bq[4];
      bq[0] = u32x4{dppu<NBC + 0>(pk), dppu<NBC + 1>(pk), dppu<NBC + 2>(pk), dppu<NBC + 3>(pk)};
      bq[1] = u32x4{dppu<NBC + 4>(pk), dppu<NBC + 5>(pk), dppu<NBC + 6>(pk), dppu<NBC + 7>(pk)};
      bq[2] = u32x4{dppu<NBC + 8>(pk), dppu<NBC + 9>(pk), dppu<NBC + 10>(pk), dppu<NBC + 11>(pk)};
      bq[3] = u32x4{dppu<NBC + 12>(pk), dppu<NBC + 13>(pk), dppu<NBC + 14>(pk), dppu<NBC + 15>(pk)};
      f32x4 a0 = zero4, a1 = zero4;
#pragma unroll
      for (int ks = 0; ks < 4; ++ks) { a0 = mfma32(auk[0][ks], bq[ks], a0); a1 = mfma32(auk[1][ks], bq[ks], a1); }
      const float sc = 0.08838834764831845f * 1.4426950408889634f;
      f32x4 a = (j & 1) ? a1 : a0;
      a *= sc;
      if (j < 2) st_dev(rs, WS_R2 + h * 2048 + (4 * cq + w) * 128 + (4 * j + q) * 16, granule(a, tag));
    }
    BAR();
    const int jq = cq, g = h >> 3;
#pragma unroll
    for (int n = 0; n < 4; ++n) {
      int row = 64 * w + 16 * n + (l >> 2);
      dma16(wuv, (u32)((row * 32 + h) * 256 + jq * 64 + (l & 3) * 16), lds0 + L_WUV + (64 * w + 16 * n) * 64);
    }
#pragma unroll
    for (int n = 0; n < 5; ++n)
      dma16(wo, (u32)((h * 128 + 32 * jq) * 1280 + (5 * w + n) * 1024 + l * 16), lds0 + L_WO + (5 * w + n) * 1024);
    if (w == 0) {
      u32 off[1] = {WS_FL + (u32)(g * 32 + (l & 31)) * 128}; bool act[1] = {true}; u32x4 v[1];
      poll_tagged<1>(rs, off, act, tag, v);
    }
    WAITVM(0);
    BAR();
    u32x4 buv[2][2];
    {
      const int rr = j >> 2, p = j & 3;
      const int c0 = 64 * w + 16 * (q >> 1) + 2 * (q & 1) + 32 * (rr & 1) + 4 * (rr >> 1);
#pragma unroll
      for (int dt = 0; dt < 2; ++dt)
#pragma unroll
        for (int a = 0; a < 2; ++a) {
          const u8* p0 = smem + L_WUV + (c0 + a) * 64 + dt * 32 + p * 8;
          buv[dt][a] = cat(trrd(p0), trrd(p0 + 8 * 64));
        }
    }
    u32x4 awo[3][2];
    {
      const int rr = j >> 2, p = j & 3;
#pragma unroll
      for (int pi = 0; pi < 3; ++pi) {
        int PP = w + 8 * pi; PP = PP < 20 ? PP : w;
#pragma unroll
        for (int tt = 0; tt < 2; ++tt) {
          const u8* p0 = smem + L_WO + (8 * q + rr) * 1280 + (32 * PP + 16 * tt + 4 * p) * 2;
          awo[pi][tt] = cat(trrd(p0), trrd(p0 + 4 * 1280));
        }
      }
    }
    {
      u32 off[1] = {WS_ML + (u32)(g * 32 + 4 * w + (l & 3)) * 128 + (h & 7) * 16}; bool act[1] = {true}; u32x4 v[1];
      poll_tagged<1>(rs, off, act, tag, v);
      int s = 4 * w + l;
      if (l < 4) *(u32x2*)(smem + L_MLB + (s & 7) * 32 + (s >> 3) * 8) = u32x2{v[0].x, v[0].z};
    }
    BAR();
    float e[4], inv;
    {
      const f32x4 m01 = *(const f32x4*)(smem + L_MLB + (l >> 3) * 32), m23 = *(const f32x4*)(smem + L_MLB + (l >> 3) * 32 + 16);
      float mx = fmaxf(fmaxf(m01[0], m01[2]), fmaxf(m23[0], m23[2]));
      mx = fmaxf(mx, dppf<ROR8>(mx)); mx = max16(mx); mx = max32(mx);
      e[0] = __builtin_amdgcn_exp2f(m01[0] - mx); e[1] = __builtin_amdgcn_exp2f(m01[2] - mx);
      e[2] = __builtin_amdgcn_exp2f(m23[0] - mx); e[3] = __builtin_amdgcn_exp2f(m23[2] - mx);
      float L = e[0] * m01[1] + e[1] * m01[3] + e[2] * m23[1] + e[3] * m23[3];
      L += dppf<ROR8>(L); L = red16(L, L); L = red32(L, L);
      inv = 1.0f / L;
    }
    u32x4 v3[4];
    {
      const u32 o = WS_R3 + (u32)(h * 32 + (l >> 3)) * 1024 + w * 128 + (l & 7) * 16;
      for (int spin = 0; spin < SPIN_MAX; ++spin) {
        bool bad = false;
        asm volatile("" ::: "memory");
#pragma unroll
        for (int i = 0; i < 4; ++i) v3[i] = ld_dev(rs, o + i * 8192);
#pragma unroll
        for (int i = 0; i < 4; ++i) bad |= (v3[i].x == SENT) | (v3[i].y == SENT) | (v3[i].z == SENT) | (v3[i].w == SENT);
        if (!wave_any(bad)) break;
      }
    }
    {
      float acc[8];
#pragma unroll
      for (int k = 0; k < 4; ++k) { acc[2 * k] = e[0] * bflo(v3[0][k]); acc[2 * k + 1] = e[0] * bfhi(v3[0][k]); }
#pragma unroll
      for (int i = 1; i < 4; ++i)
#pragma unroll
        for (int k = 0; k < 4; ++k) { acc[2 * k] += e[i] * bflo(v3[i][k]); acc[2 * k + 1] += e[i] * bfhi(v3[i][k]); }
      float r4[4], r2[2];
#pragma unroll
      for (int k = 0; k < 4; ++k) r4[k] = red32(acc[k], acc[k + 4]);
#pragma unroll
      for (int k = 0; k < 2; ++k) r2[k] = red16(r4[k], r4[k + 2]);
      const bool ah = (l & 8) != 0;
      float keep = ah ? r2[1] : r2[0], give = ah ? r2[0] : r2[1];
      float val = (keep + dppf<ROR8>(give)) * inv;
      const u32 pk = cvtpk(val, val);
      const u32 hm = (l & 1) ? 0xffff0000u : 0x0000ffffu;
      f32x4 d0 = zero4, d1 = zero4;
#pragma unroll
      for (int a = 0; a < 2; ++a) {
        u32x4 af;
#pragma unroll
        for (int k = 0; k < 4; ++k) af[k] = pk & ((((l & 7) >> 1) == k && (int)ah == a) ? hm : 0u);
        d0 = mfma32(af, buv[0][a], d0);
        d1 = mfma32(af, buv[1][a], d1);
      }
      float s0 = (d0[0] + d0[1]) + (d0[2] + d0[3]), s1 = (d1[0] + d1[1]) + (d1[2] + d1[3]);
      float s = red32(s0, s1);
      s = red16(s, s);
      if ((q & 1) == 0) *(float*)(smem + L_CTX + (16 * (l >> 5) + j) * 32 + w * 4) = s;
    }
    BAR();
    {
      const int d = 8 * q + (j & 7);
      const f32x4 c0 = *(const f32x4*)(smem + L_CTX + d * 32), c1 = *(const f32x4*)(smem + L_CTX + d * 32 + 16);
      const f32x4 cs = c0 + c1;
      const float v = (cs[0] + cs[1]) + (cs[2] + cs[3]);
      const u32 pk = cvtpk(v, dppf<XOR1>(v));
      const u32x4 bc = {dppu<NBC + 0>(pk), dppu<NBC + 2>(pk), dppu<NBC + 4>(pk), dppu<NBC + 6>(pk)};
      const int p = h * 4 + jq;
#pragma unroll
      for (int pi = 0; pi < 3; ++pi) {
        const int PP = w + 8 * pi;
        if (PP < 20) {
          f32x4 a0 = mfma32(awo[pi][0], bc, zero4), a1 = mfma32(awo[pi][1], bc, zero4);
          f32x4 a = (j & 1) ? a1 : a0;
          if (j < 2) st_dev(rs, WS_R5 + (u32)(PP * 128 + p) * 128 + (4 * j + q) * 16, granule(a, tag));
        }
      }
    }
  } else {
    const int g = idx & 3, s = 4 * xc + (idx >> 2);
    const u32 off3 = WS_R3 + (u32)((8 * g + (j & 7)) * 32 + s) * 1024 + w * 128 + 32 * q + 16 * (j >> 3);
    st_dev(rs, off3, u32x4{SENT, SENT, SENT, SENT});
    {
      const u8* base = (const u8*)ckv + (size_t)(128 * s + 16 * w) * 1024;
#pragma unroll
      for (int n = 0; n < 16; ++n) dma16(base + n * 1024, (u32)(l * 16), lds0 + L_CKV + (16 * w + n) * CKS);
    }
    const int Q = xc + 8 * (idx >> 2);
    WAITVM(0);
    BAR();
    const u32 tag = __builtin_amdgcn_readfirstlane(epoch_v) + 1u;
    if (w == 0 && l < 8) st_dev(rs, WS_FL + (u32)(g * 32 + s) * 128 + l * 16, u32x4{tag, tag, tag, tag});
    u32x4 aqk[16];
#pragma unroll
    for (int ks = 0; ks < 16; ++ks) aqk[ks] = *(const u32x4*)(smem + L_CKV + (16 * w + j) * CKS + ks * 64 + 16 * q);
    u32x4 apv[4][4];
    {
      const int rr = j >> 2, p = j & 3;
      const u8* p0 = smem + L_CKV + (4 * q + rr) * CKS + (64 * w + 4 * p) * 2;
#pragma unroll
      for (int kt = 0; kt < 4; ++kt)
#pragma unroll
        for (int tt = 0; tt < 4; ++tt)
          apv[kt][tt] = cat(trrd(p0 + (32 * kt) * CKS + tt * 32), trrd(p0 + (32 * kt + 16) * CKS + tt * 32));
    }
    {
      u32 off[2]; bool act[2] = {true, true}; u32x4 v[2];
#pragma unroll
      for (int i = 0; i < 2; ++i) off[i] = WS_R2 + (u32)(8 * g) * 2048 + (t + 512 * i) * 16;
      poll_tagged<2>(rs, off, act, tag, v);
#pragma unroll
      for (int i = 0; i < 2; ++i) {
        int n = t + 512 * i;
        *(u32x2*)(smem + L_QL + (n >> 7) * CKS + (n & 127) * 8) = u32x2{v[i].x, v[i].z};
      }
    }
    BAR();
    f32x4 sc;
    {
      f32x4 lo = zero4, hi = zero4;
      const u8* qb = smem + L_QL + (j & 7) * CKS + (j >> 3) * 512 + 16 * q;
#pragma unroll
      for (int u = 0; u < 8; ++u) {
        const u32x4 bq = *(const u32x4*)(qb + u * 64);
        lo = mfma32(aqk[u], bq, lo);
        hi = mfma32(aqk[u + 8], bq, hi);
      }
#pragma unroll
      for (int e = 0; e < 4; ++e) { float v = (j < 8) ? lo[e] : hi[e]; sc[e] = v + dppf<ROR8>(v); }
    }
    {
      float m = fmaxf(fmaxf(sc[0], sc[1]), fmaxf(sc[2], sc[3]));
      m = max16(m); m = max32(m);
      if (l < 8) *(float*)(smem + L_WM + l * 32 + w * 4) = m;
    }
    BAR();
    float mx;
    {
      const f32x4 a = *(const f32x4*)(smem + L_WM + (j & 7) * 32), c = *(const f32x4*)(smem + L_WM + (j & 7) * 32 + 16);
      mx = fmaxf(fmaxf(fmaxf(a[0], a[1]), fmaxf(a[2], a[3])), fmaxf(fmaxf(c[0], c[1]), fmaxf(c[2], c[3])));
      float p0 = __builtin_amdgcn_exp2f(sc[0] - mx), p1 = __builtin_amdgcn_exp2f(sc[1] - mx);
      float p2 = __builtin_amdgcn_exp2f(sc[2] - mx), p3 = __builtin_amdgcn_exp2f(sc[3] - mx);
      *(u32x2*)(smem + L_PF + (w >> 1) * 1024 + l * 16 + (w & 1) * 8) = u32x2{cvtpk(p0, p1), cvtpk(p2, p3)};
      float ls = (p0 + p1) + (p2 + p3);
      ls = red16(ls, ls); ls = red32(ls, ls);
      if (l < 8) *(float*)(smem + L_LP + l * 32 + w * 4) = ls;
    }
    BAR();
    {
      u32x4 pf[4];
#pragma unroll
      for (int kt = 0; kt < 4; ++kt) pf[kt] = *(const u32x4*)(smem + L_PF + kt * 1024 + l * 16);
      if (w == 0) {
        const f32x4 a = *(const f32x4*)(smem + L_LP + (j & 7) * 32), c = *(const f32x4*)(smem + L_LP + (j & 7) * 32 + 16);
        const f32x4 u = a + c;
        const float lt = (u[0] + u[1]) + (u[2] + u[3]);
        if (l < 8) st_dev(rs, WS_ML + (u32)(g * 32 + s) * 128 + l * 16, u32x4{__float_as_uint(mx), tag, __float_as_uint(lt), tag});
      }
      f32x4 o[4] = {zero4, zero4, zero4, zero4};
#pragma unroll
      for (int kt = 0; kt < 4; ++kt)
#pragma unroll
        for (int tt = 0; tt < 4; ++tt) o[tt] = mfma32(apv[kt][tt], pf[kt], o[tt]);
      const bool hb = j >= 8;
      const f32x4 oa = hb ? o[2] : o[0], ob = hb ? o[3] : o[1];
      u32x4 pv = {cvtpk(oa[0], oa[1]), cvtpk(oa[2], oa[3]), cvtpk(ob[0], ob[1]), cvtpk(ob[2], ob[3])};
#pragma unroll
      for (int k = 0; k < 4; ++k) pv[k] = pv[k] < 0xFFFFFFFEu ? pv[k] : 0xFFFFFFFEu;
      st_dev(rs, off3, pv);
    }
    if (Q < 27) {
      const int fg = 4 * Q + (idx & 3);
#pragma unroll
      for (int n = 0; n < 3; ++n) {
        const int it = w + 8 * n;
        if (it < 20) {
          const u32 vo = (u32)((32 * it + (l >> 1)) * 3456 + fg * 32 + (l & 1) * 16);
          dma16(wg, vo, lds0 + L_GATE + it * 1024);
          dma16(wu, vo, lds0 + L_UP + it * 1024);
          dma16(wd, (u32)(fg * 16 * 1280 + it * 1024 + l * 16), lds0 + L_DOWN + it * 1024);
        }
      }
      if (t < 320) *(u32*)(smem + L_ONES + t * 4) = 0x3F803F80u;
      WAITVM(0);
      BAR();
      const int rr = j >> 2, p = j & 3;
      u32x4 agu[2][5];
      if (w < 4) {
#pragma unroll
        for (int mat = 0; mat < 2; ++mat)
#pragma unroll
          for (int kk = 0; kk < 5; ++kk) {
            const u8* p0 = smem + (mat ? L_UP : L_GATE) + (32 * (5 * w + kk) + 8 * q + rr) * 32 + p * 8;
            agu[mat][kk] = cat(trrd(p0), trrd(p0 + 4 * 32));
          }
      }
      u32x2 adn[3][2];
#pragma unroll
      for (int pi = 0; pi < 3; ++pi) {
        int PP = w + 8 * pi; PP = PP < 20 ? PP : w;
#pragma unroll
        for (int tt = 0; tt < 2; ++tt) adn[pi][tt] = trrd(smem + L_DOWN + (4 * q + rr) * 1280 + (32 * PP + 16 * tt + 4 * p) * 2);
      }
      if (w < 3) {
        u32 off[1] = {WS_R6 + (u32)t * 16}; bool act[1] = {t < 160}; u32x4 v[1];
        poll_tagged<1>(rs, off, act, tag, v);
        if (t < 160) *(u32x2*)(smem + L_HB + t * 8) = u32x2{v[0].x, v[0].z};
      }
      BAR();
      if (w < 4) {
        f32x4 ga = zero4, ua = zero4;
        const u8* hbp = smem + ((j == 1) ? L_ONES : L_HB) + (160 * w + 8 * q) * 2;
#pragma unroll
        for (int kk = 0; kk < 5; ++kk) {
          const u32x4 bh = *(const u32x4*)(hbp + kk * 64);
          ga = mfma32(agu[0][kk], bh, ga);
          ua = mfma32(agu[1][kk], bh, ua);
        }
        if (j < 2) {
#pragma unroll
          for (int e = 0; e < 4; ++e) {
            *(float*)(smem + L_GP + ((0 + j) * 16 + 4 * q + e) * 16 + w * 4) = ga[e];
            *(float*)(smem + L_GP + ((2 + j) * 16 + 4 * q + e) * 16 + w * 4) = ua[e];
          }
        }
      } else {
        const float K = bflo(*(const u16*)(smem + L_HB));
        const u32x2 hv = *(const u32x2*)(smem + L_HB + (160 * (w - 4) + 4 * (l < 40 ? l : 0)) * 2);
        float d0 = bflo(hv.x) - K, d1 = bfhi(hv.x) - K, d2 = bflo(hv.y) - K, d3 = bfhi(hv.y) - K;
        float s1 = (d0 + d1) + (d2 + d3), s2 = (d0 * d0 + d1 * d1) + (d2 * d2 + d3 * d3);
        if (l >= 40) { s1 = 0.f; s2 = 0.f; }
        s1 = wave_sum(s1); s2 = wave_sum(s2);
        if (l == 0) {
          *(f32x2*)(smem + L_ST + (w - 4) * 8) = f32x2{s1, s2};
          if (w == 4) *(float*)(smem + L_ST + 32) = K;
        }
      }
      BAR();
      {
        const int mat = (j >> 2) & 1, f = 4 * q + (j & 3);
        const f32x4 hw = *(const f32x4*)(smem + L_GP + ((2 * mat + 0) * 16 + f) * 16);
        const f32x4 cs = *(const f32x4*)(smem + L_GP + ((2 * mat + 1) * 16 + f) * 16);
        const f32x4 s01 = *(const f32x4*)(smem + L_ST), s23 = *(const f32x4*)(smem + L_ST + 16);
        const float K = *(const float*)(smem + L_ST + 32);
        const float m1 = ((s01[0] + s01[2]) + (s23[0] + s23[2])) * (1.0f / 640.0f);
        const float m2 = ((s01[1] + s01[3]) + (s23[1] + s23[3])) * (1.0f / 640.0f);
        const float mean = K + m1;
        const float rstd = 1.0f / sqrtf(fmaxf(m2 - m1 * m1, 0.f) + 1e-5f);
        const float val = rstd * (((hw[0] + hw[1]) + (hw[2] + hw[3])) - mean * ((cs[0] + cs[1]) + (cs[2] + cs[3])));
        const float up = dppf<SHL4>(val);
        const float hh = (val > 0.f ? val : 0.f) * up;
        const u32 pk = cvtpk(hh, dppf<XOR1>(hh));
        const u32x2 bd = {dppu<NBC + 0>(pk), dppu<NBC + 2>(pk)};
#pragma unroll
        for (int pi = 0; pi < 3; ++pi) {
          const int PP = w + 8 * pi;
          if (PP < 20) {
            f32x4 a0 = mfma16(adn[pi][0], bd, zero4), a1 = mfma16(adn[pi][1], bd, zero4);
            f32x4 a = (j & 1) ? a1 : a0;
            if (j < 2) st_dev(rs, WS_R7 + (u32)(PP * 108 + fg) * 128 + (4 * j + q) * 16, granule(a, tag));
          }
        }
      }
    } else {
      const int cg = 4 * (Q - 27) + (idx & 3);
      const u32x2 xv = *(const u32x2*)((const u8*)x + (32 * cg + 4 * (l & 7)) * 2);
      f32x4 hsum = reduce_lines<128>(rs, WS_R5 + (u32)cg * 128 * 128, tag, smem + L_RED5, t, w, l);
      f32x4 hv = {bflo(xv.x) + hsum[0], bfhi(xv.x) + hsum[1], bflo(xv.y) + hsum[2], bfhi(xv.y) + hsum[3]};
      if (w == 0 && l < 8) st_dev(rs, WS_R6 + (u32)cg * 128 + l * 16, granule(hv, tag));
      f32x4 fsum = reduce_lines<108>(rs, WS_R7 + (u32)cg * 108 * 128, tag, smem + L_RED7, t, w, l);
      if (w == 0 && l < 8) {
        hv += fsum;
        *(u32x2*)((u8*)out + (32 * cg + 4 * l) * 2) = u32x2{cvtpk(hv[0], hv[1]), cvtpk(hv[2], hv[3])};
        if (cg == 0 && l == 0) __builtin_amdgcn_raw_buffer_store_b32(tag, rs, 0, 0, 16);
      }
    }
  }
}

extern "C" void kernel_launch(void* const* d_in, const int* in_sizes, int n_in, void* d_out, int out_size, void* d_ws,
                              size_t ws_size, hipStream_t stream) {
  (void)in_sizes; (void)n_in; (void)out_size; (void)ws_size;
  mla_ffn_decode_kernel<<<256, 512, 0, stream>>>((const u16*)d_in[0], (const u16*)d_in[1], (const u16*)d_in[2],
                                                 (const u16*)d_in[3], (const u16*)d_in[4], (const u16*)d_in[5],
                                                 (const u16*)d_in[7], (const u16*)d_in[8], (const u16*)d_in[9],
                                                 (u16*)d_out, (u8*)d_ws);
}
```

```cpp
#include <hip/hip_runtime.h>
#include <hip/hip_bf16.h>

typedef unsigned int u32;
typedef unsigned short u16;
typedef unsigned char u8;
typedef float f32x4 __attribute__((ext_vector_type(4)));
typedef float f32x2 __attribute__((ext_vector_type(2)));
typedef u32 u32x4 __attribute__((ext_vector_type(4)));
typedef u32 u32x2 __attribute__((ext_vector_type(2)));
typedef __bf16 bf16x8 __attribute__((ext_vector_type(8)));
typedef __bf16 bf16x2 __attribute__((ext_vector_type(2)));
typedef short s16x4 __attribute__((ext_vector_type(4)));
typedef __amdgpu_buffer_rsrc_t rsrc_t;

#define DEV __device__ __forceinline__

constexpr u32 WS_R2 = 4096;
constexpr u32 WS_FL = WS_R2 + 65536;
constexpr u32 WS_ML = WS_FL + 16384;
constexpr u32 WS_R6 = WS_ML + 16384;
constexpr u32 WS_R5 = WS_R6 + 4096;
constexpr u32 WS_R7 = WS_R5 + 20 * 128 * 128;
constexpr u32 WS_R3 = WS_R7 + 20 * 108 * 128 + 2048;
constexpr u32 WS_END = WS_R3 + 32 * 32 * 1024;

constexpr u32 L_WUK = 0, L_WUV = 32768, L_WO = 65536, L_QP = 106496, L_MLB = 114688, L_CTX = 114944;
constexpr u32 CKS = 1056;
constexpr u32 L_CKV = 0, L_QL = 128 * CKS, L_WM = L_QL + 8 * CKS, L_LP = L_WM + 256, L_PF = L_LP + 256;
constexpr u32 LDS_BYTES = L_PF + 4096;
constexpr u32 L_GATE = 0, L_UP = 20480, L_DOWN = 40960, L_ONES = 61440, L_HB = 62720, L_GP = 64000, L_ST = 65024;
constexpr u32 L_RED5 = 0, L_RED7 = 1024;

constexpr int SPIN_MAX = 1 << 14;
constexpr u32 SENT = 0xFFFFFFFFu;

DEV f32x4 mfma32(u32x4 a, u32x4 b, f32x4 c) {
  return __builtin_amdgcn_mfma_f32_16x16x32_bf16(__builtin_bit_cast(bf16x8, a), __builtin_bit_cast(bf16x8, b), c, 0, 0, 0);
}
DEV f32x4 mfma16(u32x2 a, u32x2 b, f32x4 c) {
  return __builtin_amdgcn_mfma_f32_16x16x16bf16_1k(__builtin_bit_cast(s16x4, a), __builtin_bit_cast(s16x4, b), c, 0, 0, 0);
}
DEV u32 cvtpk(float lo, float hi) {
  f32x2 v = {lo, hi};
  return __builtin_bit_cast(u32, __builtin_convertvector(v, bf16x2));
}
DEV float bflo(u32 u) { return __uint_as_float(u << 16); }
DEV float bfhi(u32 u) { return __uint_as_float(u & 0xffff0000u); }
template <int CTRL> DEV u32 dppu(u32 v) { return (u32)__builtin_amdgcn_update_dpp(0, (int)v, CTRL, 0xf, 0xf, true); }
template <int CTRL> DEV float dppf(float v) { return __uint_as_float(dppu<CTRL>(__float_as_uint(v))); }
constexpr int ROR8 = 0x128, ROR4 = 0x124, ROR2 = 0x122, ROR1 = 0x121, XOR1 = 0xB1, SHL4 = 0x104, NBC = 0x150;
DEV float red32(float a, float b) {
  auto s = __builtin_amdgcn_permlane32_swap(__float_as_uint(a), __float_as_uint(b), false, false);
  return __uint_as_float(s[0]) + __uint_as_float(s[1]);
}
DEV float red16(float a, float b) {
  auto s = __builtin_amdgcn_permlane16_swap(__float_as_uint(a), __float_as_uint(b), false, false);
  return __uint_as_float(s[0]) + __uint_as_float(s[1]);
}
DEV float max32(float a) {
  auto s = __builtin_amdgcn_permlane32_swap(__float_as_uint(a), __float_as_uint(a), false, false);
  return fmaxf(__uint_as_float(s[0]), __uint_as_float(s[1]));
}
DEV float max16(float a) {
  auto s = __builtin_amdgcn_permlane16_swap(__float_as_uint(a), __float_as_uint(a), false, false);
  return fmaxf(__uint_as_float(s[0]), __uint_as_float(s[1]));
}
DEV float wave_sum(float v) {
  v += dppf<ROR8>(v); v += dppf<ROR4>(v); v += dppf<ROR2>(v); v += dppf<ROR1>(v);
  v = red16(v, v);
  return red32(v, v);
}
DEV u32x2 trrd(const u8* p) {
  typedef short v4i16_t __attribute__((ext_vector_type(4)));
  return __builtin_bit_cast(u32x2, __builtin_amdgcn_ds_read_tr16_b64_v4i16((__attribute__((address_space(3))) v4i16_t*)p));
}
DEV u32x4 cat(u32x2 a, u32x2 b) { return u32x4{a.x, a.y, b.x, b.y}; }
DEV void dma16(const void* sbase, u32 voff, u32 lds) {
  u32 keep;
  asm volatile("s_mov_b32 %0, m0\n\ts_mov_b32 m0, %3\n\ts_nop 0\n\tglobal_load_lds_dwordx4 %1, %2\n\ts_mov_b32 m0, %0"
               : "=&s"(keep) : "v"(voff), "s"(sbase), "s"(lds) : "memory");
}
#define BAR() asm volatile("s_waitcnt lgkmcnt(0)\n\ts_barrier" ::: "memory")
#define WAITVM(n) asm volatile("s_waitcnt vmcnt(" #n ")" ::: "memory")
DEV u32x4 ld_dev(rsrc_t rs, u32 off) { return __builtin_amdgcn_raw_buffer_load_b128(rs, off, 0, 16); }
DEV void st_dev(rsrc_t rs, u32 off, u32x4 v) { __builtin_amdgcn_raw_buffer_store_b128(v, rs, off, 0, 16); }
DEV bool wave_any(bool p) { return __builtin_amdgcn_ballot_w64(p) != 0; }

template <int N> DEV void poll_tagged(rsrc_t rs, const u32 (&off)[N], const bool (&act)[N], u32 tag, u32x4 (&v)[N]) {
  for (int spin = 0; spin < SPIN_MAX; ++spin) {
    bool bad = false;
    asm volatile("" ::: "memory");
#pragma unroll
    for (int i = 0; i < N; ++i) {
      v[i] = u32x4{0, tag, 0, tag};
      if (act[i]) v[i] = ld_dev(rs, off[i]);
    }
#pragma unroll
    for (int i = 0; i < N; ++i) bad |= (v[i].y != tag) | (v[i].w != tag);
    if (!wave_any(bad)) break;
  }
}
DEV u32x4 granule(f32x4 a, u32 tag) { return u32x4{cvtpk(a[0], a[1]), tag, cvtpk(a[2], a[3]), tag}; }

template <int NP> DEV f32x4 reduce_lines(rsrc_t rs, u32 base, u32 tag, u8* red, int t, int w, int l) {
  u32 off[2]; bool act[2]; u32x4 v[2];
#pragma unroll
  for (int i = 0; i < 2; ++i) {
    int p = (t >> 3) + 64 * i;
    act[i] = p < NP;
    off[i] = base + p * 128 + (t & 7) * 16;
  }
  poll_tagged<2>(rs, off, act, tag, v);
  float a0 = bflo(v[0].x) + bflo(v[1].x), a1 = bfhi(v[0].x) + bfhi(v[1].x);
  float a2 = bflo(v[0].z) + bflo(v[1].z), a3 = bfhi(v[0].z) + bfhi(v[1].z);
  float r0 = red32(a0, a2), r1 = red32(a1, a3);
  float r = red16(r0, r1);
  r += dppf<ROR8>(r);
  if ((l & 8) == 0) *(float*)(red + w * 128 + (4 * (l & 7) + (l >> 4)) * 4) = r;
  BAR();
  f32x4 s = {0.f, 0.f, 0.f, 0.f};
  if (w == 0) {
#pragma unroll
    for (int i = 0; i < 8; ++i) s += *(const f32x4*)(red + i * 128 + (l & 7) * 16);
  }
  return s;
}

__global__ void tiny_kernel(unsigned long long* p) {
  const unsigned long long tstart = *(volatile unsigned long long*)p;
  int n = 0;
  while (__builtin_amdgcn_s_memrealtime() - tstart < 4000ull && ++n < 1000000) {}
}
__global__ void __launch_bounds__(512, 2)
mla_ffn_decode_kernel(const u16* __restrict__ x, const u16* __restrict__ ckv, const u16* __restrict__ wq,
                      const u16* __restrict__ wuk, const u16* __restrict__ wuv, const u16* __restrict__ wo,
                      const u16* __restrict__ wg, const u16* __restrict__ wu, const u16* __restrict__ wd,
                      u16* __restrict__ out, u8* __restrict__ ws) {
  __shared__ __attribute__((aligned(16))) u8 smem[LDS_BYTES];
  unsigned long long tstart;
  asm volatile("s_memrealtime %0\n\ts_waitcnt lgkmcnt(0)" : "=s"(tstart) :: "memory");
  if (blockIdx.x == 255 && threadIdx.x == 0) *(unsigned long long*)(ws + WS_END + 1024) = tstart;
  const u32 lds0 = (u32)(size_t)(__attribute__((address_space(3))) u8*)smem;
  const int b = blockIdx.x, xc = b & 7, r = b >> 3, idx = r & 15;
  const int t = threadIdx.x, l = t & 63, q = l >> 4, j = l & 15;
  const int w = __builtin_amdgcn_readfirstlane(t >> 6);
  const rsrc_t rs = __builtin_amdgcn_make_buffer_rsrc((void*)ws, 0, WS_END, 0x00020000);
  const u32 epoch_v = __builtin_amdgcn_raw_buffer_load_b32(rs, 0, 0, 16);
  const f32x4 zero4 = {0.f, 0.f, 0.f, 0.f};

  if (r < 16) {
    const int h = 4 * xc + (idx >> 2), cq = idx & 3;
    if (w < 4) {
#pragma unroll
      for (int n = 0; n < 8; ++n) {
        int row = 32 * w + 4 * n + q;
        dma16(wuk, (u32)(((128 * cq + row) * 32 + h) * 256 + ((j ^ (row & 15)) * 16)), lds0 + L_WUK + (32 * w + 4 * n) * 256);
      }
    }
    const u32x4 xa = *(const u32x4*)((const u8*)x + l * 16);
    const u32 xb = *(const u32*)((const u8*)x + 1024 + l * 4);
    u32x2 xr[5];
#pragma unroll
    for (int k = 0; k < 5; ++k) xr[k] = *(const u32x2*)((const u8*)x + (k * 128 + (4 * w + q) * 4) * 2);
    u32x4 wv[20];
    {
      const u8* base = (const u8*)wq + h * 256;
      const u32 vo = (u32)((4 * w + q) * 4 * 8192 + j * 16);
#pragma unroll
      for (int i = 0; i < 20; ++i) wv[i] = *(const u32x4*)(base + ((i >> 2) * 128 + (i & 3)) * 8192 + vo);
    }
    __builtin_amdgcn_sched_barrier(0);
    WAITVM(27);
    u32x4 auk[2][4];
    if (w < 4) {
#pragma unroll
      for (int tt = 0; tt < 2; ++tt)
#pragma unroll
        for (int ks = 0; ks < 4; ++ks)
          auk[tt][ks] = *(const u32x4*)(smem + L_WUK + (32 * w + 16 * tt + j) * 256 + (((4 * ks + q) ^ j) * 16));
    }
    float mean, rstd;
    {
      float v[10] = {bflo(xa.x), bfhi(xa.x), bflo(xa.y), bfhi(xa.y), bflo(xa.z), bfhi(xa.z), bflo(xa.w), bfhi(xa.w), bflo(xb), bfhi(xb)};
      float s = 0.f;
#pragma unroll
      for (int i = 0; i < 10; ++i) s += v[i];
      mean = wave_sum(s) * (1.0f / 640.0f);
      float s2 = 0.f;
#pragma unroll
      for (int i = 0; i < 10; ++i) { float d = v[i] - mean; s2 += d * d; }
      rstd = 1.0f / sqrtf(wave_sum(s2) * (1.0f / 640.0f) + 1e-5f);
    }
    {
      u32 sel[4][2];
#pragma unroll
      for (int k = 0; k < 4; ++k) {
        bool own = ((j & 7) >> 1) == k;
        sel[k][0] = own ? ((j & 1) ? 0x01000c0cu : 0x0c0c0100u) : 0x0c0c0c0cu;
        sel[k][1] = own ? ((j & 1) ? 0x03020c0cu : 0x0c0c0302u) : 0x0c0c0c0cu;
      }
      f32x4 acc0 = zero4, acc1 = zero4;
#pragma unroll
      for (int i = 0; i < 20; ++i) {
        u32 xw = (i & 2) ? xr[i >> 2].y : xr[i >> 2].x;
        xw = (j < 8) ? xw : 0x3F803F80u;
        u32x4 bf;
#pragma unroll
        for (int k = 0; k < 4; ++k) bf[k] = __builtin_amdgcn_perm(xw, xw, sel[k][i & 1]);
        if (i & 1) acc1 = mfma32(wv[i], bf, acc1); else acc0 = mfma32(wv[i], bf, acc0);
      }
      acc0 += acc1;
#pragma unroll
      for (int e = 0; e < 4; ++e) {
        int P = 16 * q + 4 * e + ((j & 7) >> 1);
        int pos = P ^ ((q & 1) << 3);
        *(float*)(smem + L_QP + w * 1024 + pos * 16 + (j >> 3) * 8 + (j & 1) * 4) = acc0[e];
      }
    }
    BAR();
    const u32 tag = __builtin_amdgcn_readfirstlane(epoch_v) + 1u;
    if (w < 4) {
      const int P = 16 * (j >> 2) + 4 * q + (j & 3);
      const int pos = P ^ (((j >> 2) & 1) << 3);
      f32x4 s = zero4;
#pragma unroll
      for (int i = 0; i < 8; ++i) s += *(const f32x4*)(smem + L_QP + i * 1024 + pos * 16);
      const u32 pk = cvtpk(rstd * (s[0] - mean * s[2]), rstd * (s[1] - mean * s[3]));
      u32x4 bq[4];
      bq[0] = u32x4{dppu<NBC + 0>(pk), dppu<NBC + 1>(pk), dppu<NBC + 2>(pk), dppu<NBC + 3>(pk)};
      bq[1] = u32x4{dppu<NBC + 4>(pk), dppu<NBC + 5>(pk), dppu<NBC + 6>(pk), dppu<NBC + 7>(pk)};
      bq[2] = u32x4{dppu<NBC + 8>(pk), dppu<NBC + 9>(pk), dppu<NBC + 10>(pk), dppu<NBC + 11>(pk)};
      bq[3] = u32x4{dppu<NBC + 12>(pk), dppu<NBC + 13>(pk), dppu<NBC + 14>(pk), dppu<NBC + 15>(pk)};
      f32x4 a0 = zero4, a1 = zero4;
#pragma unroll
      for (int ks = 0; ks < 4; ++ks) { a0 = mfma32(auk[0][ks], bq[ks], a0); a1 = mfma32(auk[1][ks], bq[ks], a1); }
      const float sc = 0.08838834764831845f * 1.4426950408889634f;
      f32x4 a = (j & 1) ? a1 : a0;
      a *= sc;
      if (j < 2) st_dev(rs, WS_R2 + h * 2048 + (4 * cq + w) * 128 + (4 * j + q) * 16, granule(a, tag));
    }
    BAR();
    const int jq = cq, g = h >> 3;
#pragma unroll
    for (int n = 0; n < 4; ++n) {
      int row = 64 * w + 16 * n + (l >> 2);
      dma16(wuv, (u32)((row * 32 + h) * 256 + jq * 64 + (l & 3) * 16), lds0 + L_WUV + (64 * w + 16 * n) * 64);
    }
#pragma unroll
    for (int n = 0; n < 5; ++n)
      dma16(wo, (u32)((h * 128 + 32 * jq) * 1280 + (5 * w + n) * 1024 + l * 16), lds0 + L_WO + (5 * w + n) * 1024);
    if (w == 0) {
      u32 off[1] = {WS_FL + (u32)(g * 32 + (l & 31)) * 128}; bool act[1] = {true}; u32x4 v[1];
      poll_tagged<1>(rs, off, act, tag, v);
    }
    WAITVM(0);
    BAR();
    u32x4 buv[2][2];
    {
      const int rr = j >> 2, p = j & 3;
      const int c0 = 64 * w + 16 * (q >> 1) + 2 * (q & 1) + 32 * (rr & 1) + 4 * (rr >> 1);
#pragma unroll
      for (int dt = 0; dt < 2; ++dt)
#pragma unroll
        for (int a = 0; a < 2; ++a) {
          const u8* p0 = smem + L_WUV + (c0 + a) * 64 + dt * 32 + p * 8;
          buv[dt][a] = cat(trrd(p0), trrd(p0 + 8 * 64));
        }
    }
    u32x4 awo[3][2];
    {
      const int rr = j >> 2, p = j & 3;
#pragma unroll
      for (int pi = 0; pi < 3; ++pi) {
        int PP = w + 8 * pi; PP = PP < 20 ? PP : w;
#pragma unroll
        for (int tt = 0; tt < 2; ++tt) {
          const u8* p0 = smem + L_WO + (8 * q + rr) * 1280 + (32 * PP + 16 * tt + 4 * p) * 2;
          awo[pi][tt] = cat(trrd(p0), trrd(p0 + 4 * 1280));
        }
      }
    }
    {
      u32 off[1] = {WS_ML + (u32)(g * 32 + 4 * w + (l & 3)) * 128 + (h & 7) * 16}; bool act[1] = {true}; u32x4 v[1];
      poll_tagged<1>(rs, off, act, tag, v);
      int s = 4 * w + l;
      if (l < 4) *(u32x2*)(smem + L_MLB + (s & 7) * 32 + (s >> 3) * 8) = u32x2{v[0].x, v[0].z};
    }
    BAR();
    float e[4], inv;
    {
      const f32x4 m01 = *(const f32x4*)(smem + L_MLB + (l >> 3) * 32), m23 = *(const f32x4*)(smem + L_MLB + (l >> 3) * 32 + 16);
      float mx = fmaxf(fmaxf(m01[0], m01[2]), fmaxf(m23[0], m23[2]));
      mx = fmaxf(mx, dppf<ROR8>(mx)); mx = max16(mx); mx = max32(mx);
      e[0] = __builtin_amdgcn_exp2f(m01[0] - mx); e[1] = __builtin_amdgcn_exp2f(m01[2] - mx);
      e[2] = __builtin_amdgcn_exp2f(m23[0] - mx); e[3] = __builtin_amdgcn_exp2f(m23[2] - mx);
      float L = e[0] * m01[1] + e[1] * m01[3] + e[2] * m23[1] + e[3] * m23[3];
      L += dppf<ROR8>(L); L = red16(L, L); L = red32(L, L);
      inv = 1.0f / L;
    }
    u32x4 v3[4];
    {
      const u32 o = WS_R3 + (u32)(h * 32 + (l >> 3)) * 1024 + w * 128 + (l & 7) * 16;
      for (int spin = 0; spin < SPIN_MAX; ++spin) {
        bool bad = false;
        asm volatile("" ::: "memory");
#pragma unroll
        for (int i = 0; i < 4; ++i) v3[i] = ld_dev(rs, o + i * 8192);
#pragma unroll
        for (int i = 0; i < 4; ++i) bad |= (v3[i].x == SENT) | (v3[i].y == SENT) | (v3[i].z == SENT) | (v3[i].w == SENT);
        if (!wave_any(bad)) break;
      }
    }
    {
      float acc[8];
#pragma unroll
      for (int k = 0; k < 4; ++k) { acc[2 * k] = e[0] * bflo(v3[0][k]); acc[2 * k + 1] = e[0] * bfhi(v3[0][k]); }
#pragma unroll
      for (int i = 1; i < 4; ++i)
#pragma unroll
        for (int k = 0; k < 4; ++k) { acc[2 * k] += e[i] * bflo(v3[i][k]); acc[2 * k + 1] += e[i] * bfhi(v3[i][k]); }
      float r4[4], r2[2];
#pragma unroll
      for (int k = 0; k < 4; ++k) r4[k] = red32(acc[k], acc[k + 4]);
#pragma unroll
      for (int k = 0; k < 2; ++k) r2[k] = red16(r4[k], r4[k + 2]);
      const bool ah = (l & 8) != 0;
      float keep = ah ? r2[1] : r2[0], give = ah ? r2[0] : r2[1];
      float val = (keep + dppf<ROR8>(give)) * inv;
      const u32 pk = cvtpk(val, val);
      const u32 hm = (l & 1) ? 0xffff0000u : 0x0000ffffu;
      f32x4 d0 = zero4, d1 = zero4;
#pragma unroll
      for (int a = 0; a < 2; ++a) {
        u32x4 af;
#pragma unroll
        for (int k = 0; k < 4; ++k) af[k] = pk & ((((l & 7) >> 1) == k && (int)ah == a) ? hm : 0u);
        d0 = mfma32(af, buv[0][a], d0);
        d1 = mfma32(af, buv[1][a], d1);
      }
      float s0 = (d0[0] + d0[1]) + (d0[2] + d0[3]), s1 = (d1[0] + d1[1]) + (d1[2] + d1[3]);
      float s = red32(s0, s1);
      s = red16(s, s);
      if ((q & 1) == 0) *(float*)(smem + L_CTX + (16 * (l >> 5) + j) * 32 + w * 4) = s;
    }
    BAR();
    {
      const int d = 8 * q + (j & 7);
      const f32x4 c0 = *(const f32x4*)(smem + L_CTX + d * 32), c1 = *(const f32x4*)(smem + L_CTX + d * 32 + 16);
      const f32x4 cs = c0 + c1;
      const float v = (cs[0] + cs[1]) + (cs[2] + cs[3]);
      const u32 pk = cvtpk(v, dppf<XOR1>(v));
      const u32x4 bc = {dppu<NBC + 0>(pk), dppu<NBC + 2>(pk), dppu<NBC + 4>(pk), dppu<NBC + 6>(pk)};
      const int p = h * 4 + jq;
#pragma unroll
      for (int pi = 0; pi < 3; ++pi) {
        const int PP = w + 8 * pi;
        if (PP < 20) {
          f32x4 a0 = mfma32(awo[pi][0], bc, zero4), a1 = mfma32(awo[pi][1], bc, zero4);
          f32x4 a = (j & 1) ? a1 : a0;
          if (j < 2) st_dev(rs, WS_R5 + (u32)(PP * 128 + p) * 128 + (4 * j + q) * 16, granule(a, tag));
        }
      }
    }
  } else {
    const int g = idx & 3, s = 4 * xc + (idx >> 2);
    const u32 off3 = WS_R3 + (u32)((8 * g + (j & 7)) * 32 + s) * 1024 + w * 128 + 32 * q + 16 * (j >> 3);
    st_dev(rs, off3, u32x4{SENT, SENT, SENT, SENT});
    {
      const u8* base = (const u8*)ckv + (size_t)(128 * s + 16 * w) * 1024;
#pragma unroll
      for (int n = 0; n < 16; ++n) dma16(base + n * 1024, (u32)(l * 16), lds0 + L_CKV + (16 * w + n) * CKS);
    }
    const int Q = xc + 8 * (idx >> 2);
    WAITVM(0);
    BAR();
    const u32 tag = __builtin_amdgcn_readfirstlane(epoch_v) + 1u;
    if (w == 0 && l < 8) st_dev(rs, WS_FL + (u32)(g * 32 + s) * 128 + l * 16, u32x4{tag, tag, tag, tag});
    u32x4 aqk[16];
#pragma unroll
    for (int ks = 0; ks < 16; ++ks) aqk[ks] = *(const u32x4*)(smem + L_CKV + (16 * w + j) * CKS + ks * 64 + 16 * q);
    u32x4 apv[4][4];
    {
      const int rr = j >> 2, p = j & 3;
      const u8* p0 = smem + L_CKV + (4 * q + rr) * CKS + (64 * w + 4 * p) * 2;
#pragma unroll
      for (int kt = 0; kt < 4; ++kt)
#pragma unroll
        for (int tt = 0; tt < 4; ++tt)
          apv[kt][tt] = cat(trrd(p0 + (32 * kt) * CKS + tt * 32), trrd(p0 + (32 * kt + 16) * CKS + tt * 32));
    }
    {
      u32 off[2]; bool act[2] = {true, true}; u32x4 v[2];
#pragma unroll
      for (int i = 0; i < 2; ++i) off[i] = WS_R2 + (u32)(8 * g) * 2048 + (t + 512 * i) * 16;
      poll_tagged<2>(rs, off, act, tag, v);
#pragma unroll
      for (int i = 0; i < 2; ++i) {
        int n = t + 512 * i;
        *(u32x2*)(smem + L_QL + (n >> 7) * CKS + (n & 127) * 8) = u32x2{v[i].x, v[i].z};
      }
    }
    BAR();
    f32x4 sc;
    {
      f32x4 lo = zero4, hi = zero4;
      const u8* qb = smem + L_QL + (j & 7) * CKS + (j >> 3) * 512 + 16 * q;
#pragma unroll
      for (int u = 0; u < 8; ++u) {
        const u32x4 bq = *(const u32x4*)(qb + u * 64);
        lo = mfma32(aqk[u], bq, lo);
        hi = mfma32(aqk[u + 8], bq, hi);
      }
#pragma unroll
      for (int e = 0; e < 4; ++e) { float v = (j < 8) ? lo[e] : hi[e]; sc[e] = v + dppf<ROR8>(v); }
    }
    {
      float m = fmaxf(fmaxf(sc[0], sc[1]), fmaxf(sc[2], sc[3]));
      m = max16(m); m = max32(m);
      if (l < 8) *(float*)(smem + L_WM + l * 32 + w * 4) = m;
    }
    BAR();
    float mx;
    {
      const f32x4 a = *(const f32x4*)(smem + L_WM + (j & 7) * 32), c = *(const f32x4*)(smem + L_WM + (j & 7) * 32 + 16);
      mx = fmaxf(fmaxf(fmaxf(a[0], a[1]), fmaxf(a[2], a[3])), fmaxf(fmaxf(c[0], c[1]), fmaxf(c[2], c[3])));
      float p0 = __builtin_amdgcn_exp2f(sc[0] - mx), p1 = __builtin_amdgcn_exp2f(sc[1] - mx);
      float p2 = __builtin_amdgcn_exp2f(sc[2] - mx), p3 = __builtin_amdgcn_exp2f(sc[3] - mx);
      *(u32x2*)(smem + L_PF + (w >> 1) * 1024 + l * 16 + (w & 1) * 8) = u32x2{cvtpk(p0, p1), cvtpk(p2, p3)};
      float ls = (p0 + p1) + (p2 + p3);
      ls = red16(ls, ls); ls = red32(ls, ls);
      if (l < 8) *(float*)(smem + L_LP + l * 32 + w * 4) = ls;
    }
    BAR();
    {
      u32x4 pf[4];
#pragma unroll
      for (int kt = 0; kt < 4; ++kt) pf[kt] = *(const u32x4*)(smem + L_PF + kt * 1024 + l * 16);
      if (w == 0) {
        const f32x4 a = *(const f32x4*)(smem + L_LP + (j & 7) * 32), c = *(const f32x4*)(smem + L_LP + (j & 7) * 32 + 16);
        const f32x4 u = a + c;
        const float lt = (u[0] + u[1]) + (u[2] + u[3]);
        if (l < 8) st_dev(rs, WS_ML + (u32)(g * 32 + s) * 128 + l * 16, u32x4{__float_as_uint(mx), tag, __float_as_uint(lt), tag});
      }
      f32x4 o[4] = {zero4, zero4, zero4, zero4};
#pragma unroll
      for (int kt = 0; kt < 4; ++kt)
#pragma unroll
        for (int tt = 0; tt < 4; ++tt) o[tt] = mfma32(apv[kt][tt], pf[kt], o[tt]);
      const bool hb = j >= 8;
      const f32x4 oa = hb ? o[2] : o[0], ob = hb ? o[3] : o[1];
      u32x4 pv = {cvtpk(oa[0], oa[1]), cvtpk(oa[2], oa[3]), cvtpk(ob[0], ob[1]), cvtpk(ob[2], ob[3])};
#pragma unroll
      for (int k = 0; k < 4; ++k) pv[k] = pv[k] < 0xFFFFFFFEu ? pv[k] : 0xFFFFFFFEu;
      st_dev(rs, off3, pv);
    }
    if (Q < 27) {
      const int fg = 4 * Q + (idx & 3);
#pragma unroll
      for (int n = 0; n < 3; ++n) {
        const int it = w + 8 * n;
        if (it < 20) {
          const u32 vo = (u32)((32 * it + (l >> 1)) * 3456 + fg * 32 + (l & 1) * 16);
          dma16(wg, vo, lds0 + L_GATE + it * 1024);
          dma16(wu, vo, lds0 + L_UP + it * 1024);
          dma16(wd, (u32)(fg * 16 * 1280 + it * 1024 + l * 16), lds0 + L_DOWN + it * 1024);
        }
      }
      if (t < 320) *(u32*)(smem + L_ONES + t * 4) = 0x3F803F80u;
      WAITVM(0);
      BAR();
      const int rr = j >> 2, p = j & 3;
      u32x4 agu[2][5];
      if (w < 4) {
#pragma unroll
        for (int mat = 0; mat < 2; ++mat)
#pragma unroll
          for (int kk = 0; kk < 5; ++kk) {
            const u8* p0 = smem + (mat ? L_UP : L_GATE) + (32 * (5 * w + kk) + 8 * q + rr) * 32 + p * 8;
            agu[mat][kk] = cat(trrd(p0), trrd(p0 + 4 * 32));
          }
      }
      u32x2 adn[3][2];
#pragma unroll
      for (int pi = 0; pi < 3; ++pi) {
        int PP = w + 8 * pi; PP = PP < 20 ? PP : w;
#pragma unroll
        for (int tt = 0; tt < 2; ++tt) adn[pi][tt] = trrd(smem + L_DOWN + (4 * q + rr) * 1280 + (32 * PP + 16 * tt + 4 * p) * 2);
      }
      if (w < 3) {
        u32 off[1] = {WS_R6 + (u32)t * 16}; bool act[1] = {t < 160}; u32x4 v[1];
        poll_tagged<1>(rs, off, act, tag, v);
        if (t < 160) *(u32x2*)(smem + L_HB + t * 8) = u32x2{v[0].x, v[0].z};
      }
      BAR();
      if (w < 4) {
        f32x4 ga = zero4, ua = zero4;
        const u8* hbp = smem + ((j == 1) ? L_ONES : L_HB) + (160 * w + 8 * q) * 2;
#pragma unroll
        for (int kk = 0; kk < 5; ++kk) {
          const u32x4 bh = *(const u32x4*)(hbp + kk * 64);
          ga = mfma32(agu[0][kk], bh, ga);
          ua = mfma32(agu[1][kk], bh, ua);
        }
        if (j < 2) {
#pragma unroll
          for (int e = 0; e < 4; ++e) {
            *(float*)(smem + L_GP + ((0 + j) * 16 + 4 * q + e) * 16 + w * 4) = ga[e];
            *(float*)(smem + L_GP + ((2 + j) * 16 + 4 * q + e) * 16 + w * 4) = ua[e];
          }
        }
      } else {
        const float K = bflo(*(const u16*)(smem + L_HB));
        const u32x2 hv = *(const u32x2*)(smem + L_HB + (160 * (w - 4) + 4 * (l < 40 ? l : 0)) * 2);
        float d0 = bflo(hv.x) - K, d1 = bfhi(hv.x) - K, d2 = bflo(hv.y) - K, d3 = bfhi(hv.y) - K;
        float s1 = (d0 + d1) + (d2 + d3), s2 = (d0 * d0 + d1 * d1) + (d2 * d2 + d3 * d3);
        if (l >= 40) { s1 = 0.f; s2 = 0.f; }
        s1 = wave_sum(s1); s2 = wave_sum(s2);
        if (l == 0) {
          *(f32x2*)(smem + L_ST + (w - 4) * 8) = f32x2{s1, s2};
          if (w == 4) *(float*)(smem + L_ST + 32) = K;
        }
      }
      BAR();
      {
        const int mat = (j >> 2) & 1, f = 4 * q + (j & 3);
        const f32x4 hw = *(const f32x4*)(smem + L_GP + ((2 * mat + 0) * 16 + f) * 16);
        const f32x4 cs = *(const f32x4*)(smem + L_GP + ((2 * mat + 1) * 16 + f) * 16);
        const f32x4 s01 = *(const f32x4*)(smem + L_ST), s23 = *(const f32x4*)(smem + L_ST + 16);
        const float K = *(const float*)(smem + L_ST + 32);
        const float m1 = ((s01[0] + s01[2]) + (s23[0] + s23[2])) * (1.0f / 640.0f);
        const float m2 = ((s01[1] + s01[3]) + (s23[1] + s23[3])) * (1.0f / 640.0f);
        const float mean = K + m1;
        const float rstd = 1.0f / sqrtf(fmaxf(m2 - m1 * m1, 0.f) + 1e-5f);
        const float val = rstd * (((hw[0] + hw[1]) + (hw[2] + hw[3])) - mean * ((cs[0] + cs[1]) + (cs[2] + cs[3])));
        const float up = dppf<SHL4>(val);
        const float hh = (val > 0.f ? val : 0.f) * up;
        const u32 pk = cvtpk(hh, dppf<XOR1>(hh));
        const u32x2 bd = {dppu<NBC + 0>(pk), dppu<NBC + 2>(pk)};
#pragma unroll
        for (int pi = 0; pi < 3; ++pi) {
          const int PP = w + 8 * pi;
          if (PP < 20) {
            f32x4 a0 = mfma16(adn[pi][0], bd, zero4), a1 = mfma16(adn[pi][1], bd, zero4);
            f32x4 a = (j & 1) ? a1 : a0;
            if (j < 2) st_dev(rs, WS_R7 + (u32)(PP * 108 + fg) * 128 + (4 * j + q) * 16, granule(a, tag));
          }
        }
      }
    } else {
      const int cg = 4 * (Q - 27) + (idx & 3);
      const u32x2 xv = *(const u32x2*)((const u8*)x + (32 * cg + 4 * (l & 7)) * 2);
      f32x4 hsum = reduce_lines<128>(rs, WS_R5 + (u32)cg * 128 * 128, tag, smem + L_RED5, t, w, l);
      f32x4 hv = {bflo(xv.x) + hsum[0], bfhi(xv.x) + hsum[1], bflo(xv.y) + hsum[2], bfhi(xv.y) + hsum[3]};
      if (w == 0 && l < 8) st_dev(rs, WS_R6 + (u32)cg * 128 + l * 16, granule(hv, tag));
      f32x4 fsum = reduce_lines<108>(rs, WS_R7 + (u32)cg * 108 * 128, tag, smem + L_RED7, t, w, l);
      if (w == 0 && l < 8) {
        hv += fsum;
        *(u32x2*)((u8*)out + (32 * cg + 4 * l) * 2) = u32x2{cvtpk(hv[0], hv[1]), cvtpk(hv[2], hv[3])};
        if (cg == 0 && l == 0) __builtin_amdgcn_raw_buffer_store_b32(tag, rs, 0, 0, 16);
      }
    }
  }
}

extern "C" void kernel_launch(void* const* d_in, const int* in_sizes, int n_in, void* d_out, int out_size, void* d_ws,
                              size_t ws_size, hipStream_t stream) {
  (void)in_sizes; (void)n_in; (void)out_size; (void)ws_size;
  mla_ffn_decode_kernel<<<256, 512, 0, stream>>>((const u16*)d_in[0], (const u16*)d_in[1], (const u16*)d_in[2],
                                                 (const u16*)d_in[3], (const u16*)d_in[4], (const u16*)d_in[5],
                                                 (const u16*)d_in[7], (const u16*)d_in[8], (const u16*)d_in[9],
                                                 (u16*)d_out, (u8*)d_ws);
  tiny_kernel<<<1, 64, 0, stream>>>((unsigned long long*)((u8*)d_ws + WS_END + 1024));
}
```

```cpp
#define EV 2
#include <hip/hip_runtime.h>
#include <hip/hip_bf16.h>

typedef unsigned int u32;
typedef unsigned short u16;
typedef unsigned char u8;
typedef float f32x4 __attribute__((ext_vector_type(4)));
typedef float f32x2 __attribute__((ext_vector_type(2)));
typedef u32 u32x4 __attribute__((ext_vector_type(4)));
typedef u32 u32x2 __attribute__((ext_vector_type(2)));
typedef __bf16 bf16x8 __attribute__((ext_vector_type(8)));
typedef __bf16 bf16x2 __attribute__((ext_vector_type(2)));
typedef short s16x4 __attribute__((ext_vector_type(4)));
typedef __amdgpu_buffer_rsrc_t rsrc_t;

#define DEV __device__ __forceinline__

constexpr u32 WS_R2 = 4096;
constexpr u32 WS_FL = WS_R2 + 65536;
constexpr u32 WS_ML = WS_FL + 16384;
constexpr u32 WS_R6 = WS_ML + 16384;
constexpr u32 WS_R5 = WS_R6 + 4096;
constexpr u32 WS_R7 = WS_R5 + 20 * 128 * 128;
constexpr u32 WS_R3 = WS_R7 + 20 * 108 * 128 + 2048;
constexpr u32 WS_END = WS_R3 + 32 * 32 * 1024;

constexpr u32 L_WUK = 0, L_WUV = 32768, L_WO = 65536, L_QP = 106496, L_MLB = 114688, L_CTX = 114944;
constexpr u32 CKS = 1056;
constexpr u32 L_CKV = 0, L_QL = 128 * CKS, L_WM = L_QL + 8 * CKS, L_LP = L_WM + 256, L_PF = L_LP + 256;
constexpr u32 LDS_BYTES = L_PF + 4096;
constexpr u32 L_GATE = 0, L_UP = 20480, L_DOWN = 40960, L_ONES = 61440, L_HB = 62720, L_GP = 64000, L_ST = 65024;
constexpr u32 L_RED5 = 0, L_RED7 = 1024;

constexpr int SPIN_MAX = 1 << 14;
constexpr u32 SENT = 0xFFFFFFFFu;

DEV f32x4 mfma32(u32x4 a, u32x4 b, f32x4 c) {
  return __builtin_amdgcn_mfma_f32_16x16x32_bf16(__builtin_bit_cast(bf16x8, a), __builtin_bit_cast(bf16x8, b), c, 0, 0, 0);
}
DEV f32x4 mfma16(u32x2 a, u32x2 b, f32x4 c) {
  return __builtin_amdgcn_mfma_f32_16x16x16bf16_1k(__builtin_bit_cast(s16x4, a), __builtin_bit_cast(s16x4, b), c, 0, 0, 0);
}
DEV u32 cvtpk(float lo, float hi) {
  f32x2 v = {lo, hi};
  return __builtin_bit_cast(u32, __builtin_convertvector(v, bf16x2));
}
DEV float bflo(u32 u) { return __uint_as_float(u << 16); }
DEV float bfhi(u32 u) { return __uint_as_float(u & 0xffff0000u); }
template <int CTRL> DEV u32 dppu(u32 v) { return (u32)__builtin_amdgcn_update_dpp(0, (int)v, CTRL, 0xf, 0xf, true); }
template <int CTRL> DEV float dppf(float v) { return __uint_as_float(dppu<CTRL>(__float_as_uint(v))); }
constexpr int ROR8 = 0x128, ROR4 = 0x124, ROR2 = 0x122, ROR1 = 0x121, XOR1 = 0xB1, SHL4 = 0x104, NBC = 0x150;
DEV float red32(float a, float b) {
  auto s = __builtin_amdgcn_permlane32_swap(__float_as_uint(a), __float_as_uint(b), false, false);
  return __uint_as_float(s[0]) + __uint_as_float(s[1]);
}
DEV float red16(float a, float b) {
  auto s = __builtin_amdgcn_permlane16_swap(__float_as_uint(a), __float_as_uint(b), false, false);
  return __uint_as_float(s[0]) + __uint_as_float(s[1]);
}
DEV float max32(float a) {
  auto s = __builtin_amdgcn_permlane32_swap(__float_as_uint(a), __float_as_uint(a), false, false);
  return fmaxf(__uint_as_float(s[0]), __uint_as_float(s[1]));
}
DEV float max16(float a) {
  auto s = __builtin_amdgcn_permlane16_swap(__float_as_uint(a), __float_as_uint(a), false, false);
  return fmaxf(__uint_as_float(s[0]), __uint_as_float(s[1]));
}
DEV float wave_sum(float v) {
  v += dppf<ROR8>(v); v += dppf<ROR4>(v); v += dppf<ROR2>(v); v += dppf<ROR1>(v);
  v = red16(v, v);
  return red32(v, v);
}
DEV u32x2 trrd(const u8* p) {
  typedef short v4i16_t __attribute__((ext_vector_type(4)));
  return __builtin_bit_cast(u32x2, __builtin_amdgcn_ds_read_tr16_b64_v4i16((__attribute__((address_space(3))) v4i16_t*)p));
}
DEV u32x4 cat(u32x2 a, u32x2 b) { return u32x4{a.x, a.y, b.x, b.y}; }
DEV void dma16(const void* sbase, u32 voff, u32 lds) {
  u32 keep;
  asm volatile("s_mov_b32 %0, m0\n\ts_mov_b32 m0, %3\n\ts_nop 0\n\tglobal_load_lds_dwordx4 %1, %2\n\ts_mov_b32 m0, %0"
               : "=&s"(keep) : "v"(voff), "s"(sbase), "s"(lds) : "memory");
}
#define STAMP(k) do { if ((k) == EV) { __builtin_amdgcn_sched_barrier(0); tev = __builtin_amdgcn_s_memrealtime(); __builtin_amdgcn_sched_barrier(0);} } while (0)
#define STAMP_FLUSH() do {} while (0)
#define BAR() asm volatile("s_waitcnt lgkmcnt(0)\n\ts_barrier" ::: "memory")
#define WAITVM(n) asm volatile("s_waitcnt vmcnt(" #n ")" ::: "memory")
DEV u32x4 ld_dev(rsrc_t rs, u32 off) { return __builtin_amdgcn_raw_buffer_load_b128(rs, off, 0, 16); }
DEV void st_dev(rsrc_t rs, u32 off, u32x4 v) { __builtin_amdgcn_raw_buffer_store_b128(v, rs, off, 0, 16); }
DEV bool wave_any(bool p) { return __builtin_amdgcn_ballot_w64(p) != 0; }

template <int N> DEV void poll_tagged(rsrc_t rs, const u32 (&off)[N], const bool (&act)[N], u32 tag, u32x4 (&v)[N]) {
  for (int spin = 0; spin < SPIN_MAX; ++spin) {
    bool bad = false;
    asm volatile("" ::: "memory");
#pragma unroll
    for (int i = 0; i < N; ++i) {
      v[i] = u32x4{0, tag, 0, tag};
      if (act[i]) v[i] = ld_dev(rs, off[i]);
    }
#pragma unroll
    for (int i = 0; i < N; ++i) bad |= (v[i].y != tag) | (v[i].w != tag);
    if (!wave_any(bad)) break;
  }
}
DEV u32x4 granule(f32x4 a, u32 tag) { return u32x4{cvtpk(a[0], a[1]), tag, cvtpk(a[2], a[3]), tag}; }

template <int NP> DEV f32x4 reduce_lines(rsrc_t rs, u32 base, u32 tag, u8* red, int t, int w, int l) {
  u32 off[2]; bool act[2]; u32x4 v[2];
#pragma unroll
  for (int i = 0; i < 2; ++i) {
    int p = (t >> 3) + 64 * i;
    act[i] = p < NP;
    off[i] = base + p * 128 + (t & 7) * 16;
  }
  poll_tagged<2>(rs, off, act, tag, v);
  float a0 = bflo(v[0].x) + bflo(v[1].x), a1 = bfhi(v[0].x) + bfhi(v[1].x);
  float a2 = bflo(v[0].z) + bflo(v[1].z), a3 = bfhi(v[0].z) + bfhi(v[1].z);
  float r0 = red32(a0, a2), r1 = red32(a1, a3);
  float r = red16(r0, r1);
  r += dppf<ROR8>(r);
  if ((l & 8) == 0) *(float*)(red + w * 128 + (4 * (l & 7) + (l >> 4)) * 4) = r;
  BAR();
  f32x4 s = {0.f, 0.f, 0.f, 0.f};
  if (w == 0) {
#pragma unroll
    for (int i = 0; i < 8; ++i) s += *(const f32x4*)(red + i * 128 + (l & 7) * 16);
  }
  return s;
}

__global__ void __launch_bounds__(512, 2)
mla_ffn_decode_kernel(const u16* __restrict__ x, const u16* __restrict__ ckv, const u16* __restrict__ wq,
                      const u16* __restrict__ wuk, const u16* __restrict__ wuv, const u16* __restrict__ wo,
                      const u16* __restrict__ wg, const u16* __restrict__ wu, const u16* __restrict__ wd,
                      u16* __restrict__ out, u8* __restrict__ ws) {
  __shared__ __attribute__((aligned(16))) u8 smem[LDS_BYTES];
  const u32 lds0 = (u32)(size_t)(__attribute__((address_space(3))) u8*)smem;
  const int b = blockIdx.x, xc = b & 7, r = b >> 3, idx = r & 15;
  const int t = threadIdx.x, l = t & 63, q = l >> 4, j = l & 15;
  const int w = __builtin_amdgcn_readfirstlane(t >> 6);
  const rsrc_t rs = __builtin_amdgcn_make_buffer_rsrc((void*)ws, 0, WS_END, 0x00020000);
  const u32 epoch_v = __builtin_amdgcn_raw_buffer_load_b32(rs, 0, 0, 16);
  const f32x4 zero4 = {0.f, 0.f, 0.f, 0.f};
  unsigned long long tev = 0;
  STAMP(0);

  if (r < 16) {
    const int h = 4 * xc + (idx >> 2), cq = idx & 3;
    if (w < 4) {
#pragma unroll
      for (int n = 0; n < 8; ++n) {
        int row = 32 * w + 4 * n + q;
        dma16(wuk, (u32)(((128 * cq + row) * 32 + h) * 256 + ((j ^ (row & 15)) * 16)), lds0 + L_WUK + (32 * w + 4 * n) * 256);
      }
    }
    const u32x4 xa = *(const u32x4*)((const u8*)x + l * 16);
    const u32 xb = *(const u32*)((const u8*)x + 1024 + l * 4);
    u32x2 xr[5];
#pragma unroll
    for (int k = 0; k < 5; ++k) xr[k] = *(const u32x2*)((const u8*)x + (k * 128 + (4 * w + q) * 4) * 2);
    u32x4 wv[20];
    {
      const u8* base = (const u8*)wq + h * 256;
      const u32 vo = (u32)((4 * w + q) * 4 * 8192 + j * 16);
#pragma unroll
      for (int i = 0; i < 20; ++i) wv[i] = *(const u32x4*)(base + ((i >> 2) * 128 + (i & 3)) * 8192 + vo);
    }
    __builtin_amdgcn_sched_barrier(0);
    STAMP(1);
    WAITVM(27);
    STAMP(2);
    u32x4 auk[2][4];
    if (w < 4) {
#pragma unroll
      for (int tt = 0; tt < 2; ++tt)
#pragma unroll
        for (int ks = 0; ks < 4; ++ks)
          auk[tt][ks] = *(const u32x4*)(smem + L_WUK + (32 * w + 16 * tt + j) * 256 + (((4 * ks + q) ^ j) * 16));
    }
    float mean, rstd;
    {
      float v[10] = {bflo(xa.x), bfhi(xa.x), bflo(xa.y), bfhi(xa.y), bflo(xa.z), bfhi(xa.z), bflo(xa.w), bfhi(xa.w), bflo(xb), bfhi(xb)};
      float s = 0.f;
#pragma unroll
      for (int i = 0; i < 10; ++i) s += v[i];
      mean = wave_sum(s) * (1.0f / 640.0f);
      float s2 = 0.f;
#pragma unroll
      for (int i = 0; i < 10; ++i) { float d = v[i] - mean; s2 += d * d; }
      rstd = 1.0f / sqrtf(wave_sum(s2) * (1.0f / 640.0f) + 1e-5f);
    }
    {
      u32 sel[4][2];
#pragma unroll
      for (int k = 0; k < 4; ++k) {
        bool own = ((j & 7) >> 1) == k;
        sel[k][0] = own ? ((j & 1) ? 0x01000c0cu : 0x0c0c0100u) : 0x0c0c0c0cu;
        sel[k][1] = own ? ((j & 1) ? 0x03020c0cu : 0x0c0c0302u) : 0x0c0c0c0cu;
      }
      f32x4 acc0 = zero4, acc1 = zero4;
#pragma unroll
      for (int i = 0; i < 20; ++i) {
        u32 xw = (i & 2) ? xr[i >> 2].y : xr[i >> 2].x;
        xw = (j < 8) ? xw : 0x3F803F80u;
        u32x4 bf;
#pragma unroll
        for (int k = 0; k < 4; ++k) bf[k] = __builtin_amdgcn_perm(xw, xw, sel[k][i & 1]);
        if (i & 1) acc1 = mfma32(wv[i], bf, acc1); else acc0 = mfma32(wv[i], bf, acc0);
      }
      acc0 += acc1;
#pragma unroll
      for (int e = 0; e < 4; ++e) {
        int P = 16 * q + 4 * e + ((j & 7) >> 1);
        int pos = P ^ ((q & 1) << 3);
        *(float*)(smem + L_QP + w * 1024 + pos * 16 + (j >> 3) * 8 + (j & 1) * 4) = acc0[e];
      }
    }
    STAMP(3);
    BAR();
    STAMP(4);
    const u32 tag = __builtin_amdgcn_readfirstlane(epoch_v) + 1u;
    if (w < 4) {
      const int P = 16 * (j >> 2) + 4 * q + (j & 3);
      const int pos = P ^ (((j >> 2) & 1) << 3);
      f32x4 s = zero4;
#pragma unroll
      for (int i = 0; i < 8; ++i) s += *(const f32x4*)(smem + L_QP + i * 1024 + pos * 16);
      const u32 pk = cvtpk(rstd * (s[0] - mean * s[2]), rstd * (s[1] - mean * s[3]));
      u32x4 bq[4];
      bq[0] = u32x4{dppu<NBC + 0>(pk), dppu<NBC + 1>(pk), dppu<NBC + 2>(pk), dppu<NBC + 3>(pk)};
      bq[1] = u32x4{dppu<NBC + 4>(pk), dppu<NBC + 5>(pk), dppu<NBC + 6>(pk), dppu<NBC + 7>(pk)};
      bq[2] = u32x4{dppu<NBC + 8>(pk), dppu<NBC + 9>(pk), dppu<NBC + 10>(pk), dppu<NBC + 11>(pk)};
      bq[3] = u32x4{dppu<NBC + 12>(pk), dppu<NBC + 13>(pk), dppu<NBC + 14>(pk), dppu<NBC + 15>(pk)};
      f32x4 a0 = zero4, a1 = zero4;
#pragma unroll
      for (int ks = 0; ks < 4; ++ks) { a0 = mfma32(auk[0][ks], bq[ks], a0); a1 = mfma32(auk[1][ks], bq[ks], a1); }
      const float sc = 0.08838834764831845f * 1.4426950408889634f;
      f32x4 a = (j & 1) ? a1 : a0;
      a *= sc;
      if (j < 2) st_dev(rs, WS_R2 + h * 2048 + (4 * cq + w) * 128 + (4 * j + q) * 16, granule(a, tag));
    }
    STAMP(5);
    BAR();
    const int jq = cq, g = h >> 3;
#pragma unroll
    for (int n = 0; n < 4; ++n) {
      int row = 64 * w + 16 * n + (l >> 2);
      dma16(wuv, (u32)((row * 32 + h) * 256 + jq * 64 + (l & 3) * 16), lds0 + L_WUV + (64 * w + 16 * n) * 64);
    }
#pragma unroll
    for (int n = 0; n < 5; ++n)
      dma16(wo, (u32)((h * 128 + 32 * jq) * 1280 + (5 * w + n) * 1024 + l * 16), lds0 + L_WO + (5 * w + n) * 1024);
    if (w == 0) {
      u32 off[1] = {WS_FL + (u32)(g * 32 + (l & 31)) * 128}; bool act[1] = {true}; u32x4 v[1];
      poll_tagged<1>(rs, off, act, tag, v);
    }
    WAITVM(0);
    BAR();
    STAMP(6);
    u32x4 buv[2][2];
    {
      const int rr = j >> 2, p = j & 3;
      const int c0 = 64 * w + 16 * (q >> 1) + 2 * (q & 1) + 32 * (rr & 1) + 4 * (rr >> 1);
#pragma unroll
      for (int dt = 0; dt < 2; ++dt)
#pragma unroll
        for (int a = 0; a < 2; ++a) {
          const u8* p0 = smem + L_WUV + (c0 + a) * 64 + dt * 32 + p * 8;
          buv[dt][a] = cat(trrd(p0), trrd(p0 + 8 * 64));
        }
    }
    u32x4 awo[3][2];
    {
      const int rr = j >> 2, p = j & 3;
#pragma unroll
      for (int pi = 0; pi < 3; ++pi) {
        int PP = w + 8 * pi; PP = PP < 20 ? PP : w;
#pragma unroll
        for (int tt = 0; tt < 2; ++tt) {
          const u8* p0 = smem + L_WO + (8 * q + rr) * 1280 + (32 * PP + 16 * tt + 4 * p) * 2;
          awo[pi][tt] = cat(trrd(p0), trrd(p0 + 4 * 1280));
        }
      }
    }
    STAMP(7);
    {
      u32 off[1] = {WS_ML + (u32)(g * 32 + 4 * w + (l & 3)) * 128 + (h & 7) * 16}; bool act[1] = {true}; u32x4 v[1];
      poll_tagged<1>(rs, off, act, tag, v);
      int s = 4 * w + l;
      if (l < 4) *(u32x2*)(smem + L_MLB + (s & 7) * 32 + (s >> 3) * 8) = u32x2{v[0].x, v[0].z};
    }
    STAMP(8);
    BAR();
    float e[4], inv;
    {
      const f32x4 m01 = *(const f32x4*)(smem + L_MLB + (l >> 3) * 32), m23 = *(const f32x4*)(smem + L_MLB + (l >> 3) * 32 + 16);
      float mx = fmaxf(fmaxf(m01[0], m01[2]), fmaxf(m23[0], m23[2]));
      mx = fmaxf(mx, dppf<ROR8>(mx)); mx = max16(mx); mx = max32(mx);
      e[0] = __builtin_amdgcn_exp2f(m01[0] - mx); e[1] = __builtin_amdgcn_exp2f(m01[2] - mx);
      e[2] = __builtin_amdgcn_exp2f(m23[0] - mx); e[3] = __builtin_amdgcn_exp2f(m23[2] - mx);
      float L = e[0] * m01[1] + e[1] * m01[3] + e[2] * m23[1] + e[3] * m23[3];
      L += dppf<ROR8>(L); L = red16(L, L); L = red32(L, L);
      inv = 1.0f / L;
    }
    u32x4 v3[4];
    {
      const u32 o = WS_R3 + (u32)(h * 32 + (l >> 3)) * 1024 + w * 128 + (l & 7) * 16;
      for (int spin = 0; spin < SPIN_MAX; ++spin) {
        bool bad = false;
        asm volatile("" ::: "memory");
#pragma unroll
        for (int i = 0; i < 4; ++i) v3[i] = ld_dev(rs, o + i * 8192);
#pragma unroll
        for (int i = 0; i < 4; ++i) bad |= (v3[i].x == SENT) | (v3[i].y == SENT) | (v3[i].z == SENT) | (v3[i].w == SENT);
        if (!wave_any(bad)) break;
      }
    }
    STAMP(9);
    {
      float acc[8];
#pragma unroll
      for (int k = 0; k < 4; ++k) { acc[2 * k] = e[0] * bflo(v3[0][k]); acc[2 * k + 1] = e[0] * bfhi(v3[0][k]); }
#pragma unroll
      for (int i = 1; i < 4; ++i)
#pragma unroll
        for (int k = 0; k < 4; ++k) { acc[2 * k] += e[i] * bflo(v3[i][k]); acc[2 * k + 1] += e[i] * bfhi(v3[i][k]); }
      float r4[4], r2[2];
#pragma unroll
      for (int k = 0; k < 4; ++k) r4[k] = red32(acc[k], acc[k + 4]);
#pragma unroll
      for (int k = 0; k < 2; ++k) r2[k] = red16(r4[k], r4[k + 2]);
      const bool ah = (l & 8) != 0;
      float keep = ah ? r2[1] : r2[0], give = ah ? r2[0] : r2[1];
      float val = (keep + dppf<ROR8>(give)) * inv;
      const u32 pk = cvtpk(val, val);
      const u32 hm = (l & 1) ? 0xffff0000u : 0x0000ffffu;
      f32x4 d0 = zero4, d1 = zero4;
#pragma unroll
      for (int a = 0; a < 2; ++a) {
        u32x4 af;
#pragma unroll
        for (int k = 0; k < 4; ++k) af[k] = pk & ((((l & 7) >> 1) == k && (int)ah == a) ? hm : 0u);
        d0 = mfma32(af, buv[0][a], d0);
        d1 = mfma32(af, buv[1][a], d1);
      }
      float s0 = (d0[0] + d0[1]) + (d0[2] + d0[3]), s1 = (d1[0] + d1[1]) + (d1[2] + d1[3]);
      float s = red32(s0, s1);
      s = red16(s, s);
      if ((q & 1) == 0) *(float*)(smem + L_CTX + (16 * (l >> 5) + j) * 32 + w * 4) = s;
    }
    STAMP(10);
    BAR();
    {
      const int d = 8 * q + (j & 7);
      const f32x4 c0 = *(const f32x4*)(smem + L_CTX + d * 32), c1 = *(const f32x4*)(smem + L_CTX + d * 32 + 16);
      const f32x4 cs = c0 + c1;
      const float v = (cs[0] + cs[1]) + (cs[2] + cs[3]);
      const u32 pk = cvtpk(v, dppf<XOR1>(v));
      const u32x4 bc = {dppu<NBC + 0>(pk), dppu<NBC + 2>(pk), dppu<NBC + 4>(pk), dppu<NBC + 6>(pk)};
      const int p = h * 4 + jq;
#pragma unroll
      for (int pi = 0; pi < 3; ++pi) {
        const int PP = w + 8 * pi;
        if (PP < 20) {
          f32x4 a0 = mfma32(awo[pi][0], bc, zero4), a1 = mfma32(awo[pi][1], bc, zero4);
          f32x4 a = (j & 1) ? a1 : a0;
          if (j < 2) st_dev(rs, WS_R5 + (u32)(PP * 128 + p) * 128 + (4 * j + q) * 16, granule(a, tag));
        }
      }
    }
    STAMP(11);
  } else {
    const int g = idx & 3, s = 4 * xc + (idx >> 2);
    const u32 off3 = WS_R3 + (u32)((8 * g + (j & 7)) * 32 + s) * 1024 + w * 128 + 32 * q + 16 * (j >> 3);
    st_dev(rs, off3, u32x4{SENT, SENT, SENT, SENT});
    {
      const u8* base = (const u8*)ckv + (size_t)(128 * s + 16 * w) * 1024;
#pragma unroll
      for (int n = 0; n < 16; ++n) dma16(base + n * 1024, (u32)(l * 16), lds0 + L_CKV + (16 * w + n) * CKS);
    }
    const int Q = xc + 8 * (idx >> 2);
    STAMP(1);
    WAITVM(0);
    BAR();
    STAMP(2);
    const u32 tag = __builtin_amdgcn_readfirstlane(epoch_v) + 1u;
    if (w == 0 && l < 8) st_dev(rs, WS_FL + (u32)(g * 32 + s) * 128 + l * 16, u32x4{tag, tag, tag, tag});
    u32x4 aqk[16];
#pragma unroll
    for (int ks = 0; ks < 16; ++ks) aqk[ks] = *(const u32x4*)(smem + L_CKV + (16 * w + j) * CKS + ks * 64 + 16 * q);
    u32x4 apv[4][4];
    {
      const int rr = j >> 2, p = j & 3;
      const u8* p0 = smem + L_CKV + (4 * q + rr) * CKS + (64 * w + 4 * p) * 2;
#pragma unroll
      for (int kt = 0; kt < 4; ++kt)
#pragma unroll
        for (int tt = 0; tt < 4; ++tt)
          apv[kt][tt] = cat(trrd(p0 + (32 * kt) * CKS + tt * 32), trrd(p0 + (32 * kt + 16) * CKS + tt * 32));
    }
    STAMP(3);
    {
      u32 off[2]; bool act[2] = {true, true}; u32x4 v[2];
#pragma unroll
      for (int i = 0; i < 2; ++i) off[i] = WS_R2 + (u32)(8 * g) * 2048 + (t + 512 * i) * 16;
      poll_tagged<2>(rs, off, act, tag, v);
#pragma unroll
      for (int i = 0; i < 2; ++i) {
        int n = t + 512 * i;
        *(u32x2*)(smem + L_QL + (n >> 7) * CKS + (n & 127) * 8) = u32x2{v[i].x, v[i].z};
      }
    }
    STAMP(4);
    BAR();
    f32x4 sc;
    {
      f32x4 lo = zero4, hi = zero4;
      const u8* qb = smem + L_QL + (j & 7) * CKS + (j >> 3) * 512 + 16 * q;
#pragma unroll
      for (int u = 0; u < 8; ++u) {
        const u32x4 bq = *(const u32x4*)(qb + u * 64);
        lo = mfma32(aqk[u], bq, lo);
        hi = mfma32(aqk[u + 8], bq, hi);
      }
#pragma unroll
      for (int e = 0; e < 4; ++e) { float v = (j < 8) ? lo[e] : hi[e]; sc[e] = v + dppf<ROR8>(v); }
    }
    {
      float m = fmaxf(fmaxf(sc[0], sc[1]), fmaxf(sc[2], sc[3]));
      m = max16(m); m = max32(m);
      if (l < 8) *(float*)(smem + L_WM + l * 32 + w * 4) = m;
    }
    STAMP(5);
    BAR();
    float mx;
    {
      const f32x4 a = *(const f32x4*)(smem + L_WM + (j & 7) * 32), c = *(const f32x4*)(smem + L_WM + (j & 7) * 32 + 16);
      mx = fmaxf(fmaxf(fmaxf(a[0], a[1]), fmaxf(a[2], a[3])), fmaxf(fmaxf(c[0], c[1]), fmaxf(c[2], c[3])));
      float p0 = __builtin_amdgcn_exp2f(sc[0] - mx), p1 = __builtin_amdgcn_exp2f(sc[1] - mx);
      float p2 = __builtin_amdgcn_exp2f(sc[2] - mx), p3 = __builtin_amdgcn_exp2f(sc[3] - mx);
      *(u32x2*)(smem + L_PF + (w >> 1) * 1024 + l * 16 + (w & 1) * 8) = u32x2{cvtpk(p0, p1), cvtpk(p2, p3)};
      float ls = (p0 + p1) + (p2 + p3);
      ls = red16(ls, ls); ls = red32(ls, ls);
      if (l < 8) *(float*)(smem + L_LP + l * 32 + w * 4) = ls;
    }
    BAR();
    {
      u32x4 pf[4];
#pragma unroll
      for (int kt = 0; kt < 4; ++kt) pf[kt] = *(const u32x4*)(smem + L_PF + kt * 1024 + l * 16);
      if (w == 0) {
        const f32x4 a = *(const f32x4*)(smem + L_LP + (j & 7) * 32), c = *(const f32x4*)(smem + L_LP + (j & 7) * 32 + 16);
        const f32x4 u = a + c;
        const float lt = (u[0] + u[1]) + (u[2] + u[3]);
        if (l < 8) st_dev(rs, WS_ML + (u32)(g * 32 + s) * 128 + l * 16, u32x4{__float_as_uint(mx), tag, __float_as_uint(lt), tag});
      }
      f32x4 o[4] = {zero4, zero4, zero4, zero4};
#pragma unroll
      for (int kt = 0; kt < 4; ++kt)
#pragma unroll
        for (int tt = 0; tt < 4; ++tt) o[tt] = mfma32(apv[kt][tt], pf[kt], o[tt]);
      const bool hb = j >= 8;
      const f32x4 oa = hb ? o[2] : o[0], ob = hb ? o[3] : o[1];
      u32x4 pv = {cvtpk(oa[0], oa[1]), cvtpk(oa[2], oa[3]), cvtpk(ob[0], ob[1]), cvtpk(ob[2], ob[3])};
#pragma unroll
      for (int k = 0; k < 4; ++k) pv[k] = pv[k] < 0xFFFFFFFEu ? pv[k] : 0xFFFFFFFEu;
      st_dev(rs, off3, pv);
    }
    STAMP(6);
    if (Q < 27) {
      const int fg = 4 * Q + (idx & 3);
#pragma unroll
      for (int n = 0; n < 3; ++n) {
        const int it = w + 8 * n;
        if (it < 20) {
          const u32 vo = (u32)((32 * it + (l >> 1)) * 3456 + fg * 32 + (l & 1) * 16);
          dma16(wg, vo, lds0 + L_GATE + it * 1024);
          dma16(wu, vo, lds0 + L_UP + it * 1024);
          dma16(wd, (u32)(fg * 16 * 1280 + it * 1024 + l * 16), lds0 + L_DOWN + it * 1024);
        }
      }
      if (t < 320) *(u32*)(smem + L_ONES + t * 4) = 0x3F803F80u;
      WAITVM(0);
      BAR();
      STAMP(7);
      const int rr = j >> 2, p = j & 3;
      u32x4 agu[2][5];
      if (w < 4) {
#pragma unroll
        for (int mat = 0; mat < 2; ++mat)
#pragma unroll
          for (int kk = 0; kk < 5; ++kk) {
            const u8* p0 = smem + (mat ? L_UP : L_GATE) + (32 * (5 * w + kk) + 8 * q + rr) * 32 + p * 8;
            agu[mat][kk] = cat(trrd(p0), trrd(p0 + 4 * 32));
          }
      }
      u32x2 adn[3][2];
#pragma unroll
      for (int pi = 0; pi < 3; ++pi) {
        int PP = w + 8 * pi; PP = PP < 20 ? PP : w;
#pragma unroll
        for (int tt = 0; tt < 2; ++tt) adn[pi][tt] = trrd(smem + L_DOWN + (4 * q + rr) * 1280 + (32 * PP + 16 * tt + 4 * p) * 2);
      }
      STAMP(8);
      if (w < 3) {
        u32 off[1] = {WS_R6 + (u32)t * 16}; bool act[1] = {t < 160}; u32x4 v[1];
        poll_tagged<1>(rs, off, act, tag, v);
        if (t < 160) *(u32x2*)(smem + L_HB + t * 8) = u32x2{v[0].x, v[0].z};
      }
      STAMP(9);
      BAR();
      if (w < 4) {
        f32x4 ga = zero4, ua = zero4;
        const u8* hbp = smem + ((j == 1) ? L_ONES : L_HB) + (160 * w + 8 * q) * 2;
#pragma unroll
        for (int kk = 0; kk < 5; ++kk) {
          const u32x4 bh = *(const u32x4*)(hbp + kk * 64);
          ga = mfma32(agu[0][kk], bh, ga);
          ua = mfma32(agu[1][kk], bh, ua);
        }
        if (j < 2) {
#pragma unroll
          for (int e = 0; e < 4; ++e) {
            *(float*)(smem + L_GP + ((0 + j) * 16 + 4 * q + e) * 16 + w * 4) = ga[e];
            *(float*)(smem + L_GP + ((2 + j) * 16 + 4 * q + e) * 16 + w * 4) = ua[e];
          }
        }
      } else {
        const float K = bflo(*(const u16*)(smem + L_HB));
        const u32x2 hv = *(const u32x2*)(smem + L_HB + (160 * (w - 4) + 4 * (l < 40 ? l : 0)) * 2);
        float d0 = bflo(hv.x) - K, d1 = bfhi(hv.x) - K, d2 = bflo(hv.y) - K, d3 = bfhi(hv.y) - K;
        float s1 = (d0 + d1) + (d2 + d3), s2 = (d0 * d0 + d1 * d1) + (d2 * d2 + d3 * d3);
        if (l >= 40) { s1 = 0.f; s2 = 0.f; }
        s1 = wave_sum(s1); s2 = wave_sum(s2);
        if (l == 0) {
          *(f32x2*)(smem + L_ST + (w - 4) * 8) = f32x2{s1, s2};
          if (w == 4) *(float*)(smem + L_ST + 32) = K;
        }
      }
      STAMP(10);
      BAR();
      {
        const int mat = (j >> 2) & 1, f = 4 * q + (j & 3);
        const f32x4 hw = *(const f32x4*)(smem + L_GP + ((2 * mat + 0) * 16 + f) * 16);
        const f32x4 cs = *(const f32x4*)(smem + L_GP + ((2 * mat + 1) * 16 + f) * 16);
        const f32x4 s01 = *(const f32x4*)(smem + L_ST), s23 = *(const f32x4*)(smem + L_ST + 16);
        const float K = *(const float*)(smem + L_ST + 32);
        const float m1 = ((s01[0] + s01[2]) + (s23[0] + s23[2])) * (1.0f / 640.0f);
        const float m2 = ((s01[1] + s01[3]) + (s23[1] + s23[3])) * (1.0f / 640.0f);
        const float mean = K + m1;
        const float rstd = 1.0f / sqrtf(fmaxf(m2 - m1 * m1, 0.f) + 1e-5f);
        const float val = rstd * (((hw[0] + hw[1]) + (hw[2] + hw[3])) - mean * ((cs[0] + cs[1]) + (cs[2] + cs[3])));
        const float up = dppf<SHL4>(val);
        const float hh = (val > 0.f ? val : 0.f) * up;
        const u32 pk = cvtpk(hh, dppf<XOR1>(hh));
        const u32x2 bd = {dppu<NBC + 0>(pk), dppu<NBC + 2>(pk)};
#pragma unroll
        for (int pi = 0; pi < 3; ++pi) {
          const int PP = w + 8 * pi;
          if (PP < 20) {
            f32x4 a0 = mfma16(adn[pi][0], bd, zero4), a1 = mfma16(adn[pi][1], bd, zero4);
            f32x4 a = (j & 1) ? a1 : a0;
            if (j < 2) st_dev(rs, WS_R7 + (u32)(PP * 108 + fg) * 128 + (4 * j + q) * 16, granule(a, tag));
          }
        }
      }
      STAMP(11);
    } else {
      const int cg = 4 * (Q - 27) + (idx & 3);
      const u32x2 xv = *(const u32x2*)((const u8*)x + (32 * cg + 4 * (l & 7)) * 2);
      f32x4 hsum = reduce_lines<128>(rs, WS_R5 + (u32)cg * 128 * 128, tag, smem + L_RED5, t, w, l);
      f32x4 hv = {bflo(xv.x) + hsum[0], bfhi(xv.x) + hsum[1], bflo(xv.y) + hsum[2], bfhi(xv.y) + hsum[3]};
      STAMP(7);
      if (w == 0 && l < 8) st_dev(rs, WS_R6 + (u32)cg * 128 + l * 16, granule(hv, tag));
      f32x4 fsum = reduce_lines<108>(rs, WS_R7 + (u32)cg * 108 * 128, tag, smem + L_RED7, t, w, l);
      STAMP(8);
      while (__builtin_amdgcn_s_memrealtime() - tev < 3000ull) {}
      if (w == 0 && l < 8) {
        hv += fsum;
        *(u32x2*)((u8*)out + (32 * cg + 4 * l) * 2) = u32x2{cvtpk(hv[0], hv[1]), cvtpk(hv[2], hv[3])};
        if (cg == 0 && l == 0) __builtin_amdgcn_raw_buffer_store_b32(tag, rs, 0, 0, 16);
      }
      STAMP(9);
    }
  }
  STAMP_FLUSH();
}

extern "C" void kernel_launch(void* const* d_in, const int* in_sizes, int n_in, void* d_out, int out_size, void* d_ws,
                              size_t ws_size, hipStream_t stream) {
  (void)in_sizes; (void)n_in; (void)out_size; (void)ws_size;
  mla_ffn_decode_kernel<<<256, 512, 0, stream>>>((const u16*)d_in[0], (const u16*)d_in[1], (const u16*)d_in[2],
                                                 (const u16*)d_in[3], (const u16*)d_in[4], (const u16*)d_in[5],
                                                 (const u16*)d_in[7], (const u16*)d_in[8], (const u16*)d_in[9],
                                                 (u16*)d_out, (u8*)d_ws);
}
```

```cpp
#include <hip/hip_runtime.h>
#include <hip/hip_bf16.h>

typedef unsigned int u32;
typedef unsigned short u16;
typedef unsigned char u8;
typedef float f32x4 __attribute__((ext_vector_type(4)));
typedef float f32x2 __attribute__((ext_vector_type(2)));
typedef u32 u32x4 __attribute__((ext_vector_type(4)));
typedef u32 u32x2 __attribute__((ext_vector_type(2)));
typedef __bf16 bf16x8 __attribute__((ext_vector_type(8)));
typedef __bf16 bf16x2 __attribute__((ext_vector_type(2)));
typedef short s16x4 __attribute__((ext_vector_type(4)));
typedef __amdgpu_buffer_rsrc_t rsrc_t;

#define DEV __device__ __forceinline__

constexpr u32 WS_R2 = 4096;
constexpr u32 WS_FL = WS_R2 + 65536;
constexpr u32 WS_ML = WS_FL + 16384;
constexpr u32 WS_R6 = WS_ML + 16384;
constexpr u32 WS_R5 = WS_R6 + 4096;
constexpr u32 WS_R7 = WS_R5 + 20 * 128 * 128;
constexpr u32 WS_R3 = WS_R7 + 20 * 108 * 128 + 2048;
constexpr u32 WS_END = WS_R3 + 32 * 32 * 1024;

constexpr u32 L_WUK = 0, L_WUV = 32768, L_WO = 65536, L_QP = 106496, L_MLB = 114688, L_CTX = 114944;
constexpr u32 CKS = 1056;
constexpr u32 L_CKV = 0, L_QL = 128 * CKS, L_WM = L_QL + 8 * CKS, L_LP = L_WM + 256, L_PF = L_LP + 256;
constexpr u32 LDS_BYTES = L_PF + 4096;
constexpr u32 L_GATE = 0, L_UP = 20480, L_DOWN = 40960, L_ONES = 61440, L_HB = 62720, L_GP = 64000, L_ST = 65024;
constexpr u32 L_RED5 = 0, L_RED7 = 1024;

constexpr int PF_LINES = 126;
constexpr int SPIN_MAX = 1 << 14;
constexpr u32 SENT = 0xFFFFFFFFu;

DEV f32x4 mfma32(u32x4 a, u32x4 b, f32x4 c) {
  return __builtin_amdgcn_mfma_f32_16x16x32_bf16(__builtin_bit_cast(bf16x8, a), __builtin_bit_cast(bf16x8, b), c, 0, 0, 0);
}
DEV f32x4 mfma16(u32x2 a, u32x2 b, f32x4 c) {
  return __builtin_amdgcn_mfma_f32_16x16x16bf16_1k(__builtin_bit_cast(s16x4, a), __builtin_bit_cast(s16x4, b), c, 0, 0, 0);
}
DEV u32 cvtpk(float lo, float hi) {
  f32x2 v = {lo, hi};
  return __builtin_bit_cast(u32, __builtin_convertvector(v, bf16x2));
}
DEV float bflo(u32 u) { return __uint_as_float(u << 16); }
DEV float bfhi(u32 u) { return __uint_as_float(u & 0xffff0000u); }
template <int CTRL> DEV u32 dppu(u32 v) { return (u32)__builtin_amdgcn_update_dpp(0, (int)v, CTRL, 0xf, 0xf, true); }
template <int CTRL> DEV float dppf(float v) { return __uint_as_float(dppu<CTRL>(__float_as_uint(v))); }
constexpr int ROR8 = 0x128, ROR4 = 0x124, ROR2 = 0x122, ROR1 = 0x121, XOR1 = 0xB1, SHL4 = 0x104, NBC = 0x150;
DEV float red32(float a, float b) {
  auto s = __builtin_amdgcn_permlane32_swap(__float_as_uint(a), __float_as_uint(b), false, false);
  return __uint_as_float(s[0]) + __uint_as_float(s[1]);
}
DEV float red16(float a, float b) {
  auto s = __builtin_amdgcn_permlane16_swap(__float_as_uint(a), __float_as_uint(b), false, false);
  return __uint_as_float(s[0]) + __uint_as_float(s[1]);
}
DEV float max32(float a) {
  auto s = __builtin_amdgcn_permlane32_swap(__float_as_uint(a), __float_as_uint(a), false, false);
  return fmaxf(__uint_as_float(s[0]), __uint_as_float(s[1]));
}
DEV float max16(float a) {
  auto s = __builtin_amdgcn_permlane16_swap(__float_as_uint(a), __float_as_uint(a), false, false);
  return fmaxf(__uint_as_float(s[0]), __uint_as_float(s[1]));
}
DEV float wave_sum(float v) {
  v += dppf<ROR8>(v); v += dppf<ROR4>(v); v += dppf<ROR2>(v); v += dppf<ROR1>(v);
  v = red16(v, v);
  return red32(v, v);
}
DEV u32x2 trrd(const u8* p) {
  typedef short v4i16_t __attribute__((ext_vector_type(4)));
  return __builtin_bit_cast(u32x2, __builtin_amdgcn_ds_read_tr16_b64_v4i16((__attribute__((address_space(3))) v4i16_t*)p));
}
DEV u32x4 cat(u32x2 a, u32x2 b) { return u32x4{a.x, a.y, b.x, b.y}; }
DEV void dma16(const void* sbase, u32 voff, u32 lds) {
  u32 keep;
  asm volatile("s_mov_b32 %0, m0\n\ts_mov_b32 m0, %3\n\ts_nop 0\n\tglobal_load_lds_dwordx4 %1, %2\n\ts_mov_b32 m0, %0"
               : "=&s"(keep) : "v"(voff), "s"(sbase), "s"(lds) : "memory");
}
#define BAR() asm volatile("s_waitcnt lgkmcnt(0)\n\ts_barrier" ::: "memory")
#define WAITVM(n) asm volatile("s_waitcnt vmcnt(" #n ")" ::: "memory")
DEV u32x4 ld_dev(rsrc_t rs, u32 off) { return __builtin_amdgcn_raw_buffer_load_b128(rs, off, 0, 16); }
DEV void st_dev(rsrc_t rs, u32 off, u32x4 v) { __builtin_amdgcn_raw_buffer_store_b128(v, rs, off, 0, 16); }
DEV bool wave_any(bool p) { return __builtin_amdgcn_ballot_w64(p) != 0; }

template <int N> DEV void poll_tagged(rsrc_t rs, const u32 (&off)[N], const bool (&act)[N], u32 tag, u32x4 (&v)[N]) {
  for (int spin = 0; spin < SPIN_MAX; ++spin) {
    bool bad = false;
    asm volatile("" ::: "memory");
#pragma unroll
    for (int i = 0; i < N; ++i) {
      v[i] = u32x4{0, tag, 0, tag};
      if (act[i]) v[i] = ld_dev(rs, off[i]);
    }
#pragma unroll
    for (int i = 0; i < N; ++i) bad |= (v[i].y != tag) | (v[i].w != tag);
    if (!wave_any(bad)) break;
  }
}
DEV u32x4 granule(f32x4 a, u32 tag) { return u32x4{cvtpk(a[0], a[1]), tag, cvtpk(a[2], a[3]), tag}; }

template <int NP> DEV f32x4 reduce_lines(rsrc_t rs, u32 base, u32 tag, u8* red, int t, int w, int l) {
  u32 off[2]; bool act[2]; u32x4 v[2];
#pragma unroll
  for (int i = 0; i < 2; ++i) {
    int p = (t >> 3) + 64 * i;
    act[i] = p < NP;
    off[i] = base + p * 128 + (t & 7) * 16;
  }
  poll_tagged<2>(rs, off, act, tag, v);
  float a0 = bflo(v[0].x) + bflo(v[1].x), a1 = bfhi(v[0].x) + bfhi(v[1].x);
  float a2 = bflo(v[0].z) + bflo(v[1].z), a3 = bfhi(v[0].z) + bfhi(v[1].z);
  float r0 = red32(a0, a2), r1 = red32(a1, a3);
  float r = red16(r0, r1);
  r += dppf<ROR8>(r);
  if ((l & 8) == 0) *(float*)(red + w * 128 + (4 * (l & 7) + (l >> 4)) * 4) = r;
  BAR();
  f32x4 s = {0.f, 0.f, 0.f, 0.f};
  if (w == 0) {
#pragma unroll
    for (int i = 0; i < 8; ++i) s += *(const f32x4*)(red + i * 128 + (l & 7) * 16);
  }
  return s;
}

__global__ void __launch_bounds__(512, 2)
mla_ffn_decode_kernel(const u16* __restrict__ x, const u16* __restrict__ ckv, const u16* __restrict__ wq,
                      const u16* __restrict__ wuk, const u16* __restrict__ wuv, const u16* __restrict__ wo,
                      const u16* __restrict__ wg, const u16* __restrict__ wu, const u16* __restrict__ wd,
                      u16* __restrict__ out, u8* __restrict__ ws) {
  __shared__ __attribute__((aligned(16))) u8 smem[LDS_BYTES + 256];
  const u32 lds0 = (u32)(size_t)(__attribute__((address_space(3))) u8*)smem;
  const int b = blockIdx.x, xc = b & 7, r = b >> 3, idx = r & 15;
  const int t = threadIdx.x, l = t & 63, q = l >> 4, j = l & 15;
  const int w = __builtin_amdgcn_readfirstlane(t >> 6);
  const rsrc_t rs = __builtin_amdgcn_make_buffer_rsrc((void*)ws, 0, WS_END, 0x00020000);
  const u32 epoch_v = __builtin_amdgcn_raw_buffer_load_b32(rs, 0, 0, 16);
  const f32x4 zero4 = {0.f, 0.f, 0.f, 0.f};
  if (w == 7) {
    u32 pf_lo, pf_hi;
    asm volatile("s_getpc_b64 s[20:21]\n\ts_mov_b32 %0, s20\n\ts_mov_b32 %1, s21" : "=s"(pf_lo), "=s"(pf_hi) : : "s20", "s21");
    const u8* pc = (const u8*)(((unsigned long long)pf_hi << 32) | (pf_lo & ~127u));
    u32 keep;
    if (l < 4 && r * 4 + l < PF_LINES)
      asm volatile("s_mov_b32 %0, m0\n\ts_mov_b32 m0, %3\n\ts_nop 0\n\tglobal_load_lds_dword %1, %2\n\ts_mov_b32 m0, %0"
               : "=&s"(keep) : "v"((u32)((r * 4 + l) * 128)), "s"(pc), "s"(lds0 + LDS_BYTES) : "memory");
  }

  if (r < 16) {
    const int h = 4 * xc + (idx >> 2), cq = idx & 3;
    if (w < 4) {
#pragma unroll
      for (int n = 0; n < 8; ++n) {
        int row = 32 * w + 4 * n + q;
        dma16(wuk, (u32)(((128 * cq + row) * 32 + h) * 256 + ((j ^ (row & 15)) * 16)), lds0 + L_WUK + (32 * w + 4 * n) * 256);
      }
    }
    const u32x4 xa = *(const u32x4*)((const u8*)x + l * 16);
    const u32 xb = *(const u32*)((const u8*)x + 1024 + l * 4);
    u32x2 xr[5];
#pragma unroll
    for (int k = 0; k < 5; ++k) xr[k] = *(const u32x2*)((const u8*)x + (k * 128 + (4 * w + q) * 4) * 2);
    u32x4 wv[20];
    {
      const u8* base = (const u8*)wq + h * 256;
      const u32 vo = (u32)((4 * w + q) * 4 * 8192 + j * 16);
#pragma unroll
      for (int i = 0; i < 20; ++i) wv[i] = *(const u32x4*)(base + ((i >> 2) * 128 + (i & 3)) * 8192 + vo);
    }
    __builtin_amdgcn_sched_barrier(0);
    WAITVM(27);
    u32x4 auk[2][4];
    if (w < 4) {
#pragma unroll
      for (int tt = 0; tt < 2; ++tt)
#pragma unroll
        for (int ks = 0; ks < 4; ++ks)
          auk[tt][ks] = *(const u32x4*)(smem + L_WUK + (32 * w + 16 * tt + j) * 256 + (((4 * ks + q) ^ j) * 16));
    }
    float mean, rstd;
    {
      float v[10] = {bflo(xa.x), bfhi(xa.x), bflo(xa.y), bfhi(xa.y), bflo(xa.z), bfhi(xa.z), bflo(xa.w), bfhi(xa.w), bflo(xb), bfhi(xb)};
      float s = 0.f;
#pragma unroll
      for (int i = 0; i < 10; ++i) s += v[i];
      mean = wave_sum(s) * (1.0f / 640.0f);
      float s2 = 0.f;
#pragma unroll
      for (int i = 0; i < 10; ++i) { float d = v[i] - mean; s2 += d * d; }
      rstd = 1.0f / sqrtf(wave_sum(s2) * (1.0f / 640.0f) + 1e-5f);
    }
    {
      u32 sel[4][2];
#pragma unroll
      for (int k = 0; k < 4; ++k) {
        bool own = ((j & 7) >> 1) == k;
        sel[k][0] = own ? ((j & 1) ? 0x01000c0cu : 0x0c0c0100u) : 0x0c0c0c0cu;
        sel[k][1] = own ? ((j & 1) ? 0x03020c0cu : 0x0c0c0302u) : 0x0c0c0c0cu;
      }
      f32x4 acc0 = zero4, acc1 = zero4;
#pragma unroll
      for (int i = 0; i < 20; ++i) {
        u32 xw = (i & 2) ? xr[i >> 2].y : xr[i >> 2].x;
        xw = (j < 8) ? xw : 0x3F803F80u;
        u32x4 bf;
#pragma unroll
        for (int k = 0; k < 4; ++k) bf[k] = __builtin_amdgcn_perm(xw, xw, sel[k][i & 1]);
        if (i & 1) acc1 = mfma32(wv[i], bf, acc1); else acc0 = mfma32(wv[i], bf, acc0);
      }
      acc0 += acc1;
#pragma unroll
      for (int e = 0; e < 4; ++e) {
        int P = 16 * q + 4 * e + ((j & 7) >> 1);
        int pos = P ^ ((q & 1) << 3);
        *(float*)(smem + L_QP + w * 1024 + pos * 16 + (j >> 3) * 8 + (j & 1) * 4) = acc0[e];
      }
    }
    BAR();
    const u32 tag = __builtin_amdgcn_readfirstlane(epoch_v) + 1u;
    if (w < 4) {
      const int P = 16 * (j >> 2) + 4 * q + (j & 3);
      const int pos = P ^ (((j >> 2) & 1) << 3);
      f32x4 s = zero4;
#pragma unroll
      for (int i = 0; i < 8; ++i) s += *(const f32x4*)(smem + L_QP + i * 1024 + pos * 16);
      const u32 pk = cvtpk(rstd * (s[0] - mean * s[2]), rstd * (s[1] - mean * s[3]));
      u32x4 bq[4];
      bq[0] = u32x4{dppu<NBC + 0>(pk), dppu<NBC + 1>(pk), dppu<NBC + 2>(pk), dppu<NBC + 3>(pk)};
      bq[1] = u32x4{dppu<NBC + 4>(pk), dppu<NBC + 5>(pk), dppu<NBC + 6>(pk), dppu<NBC + 7>(pk)};
      bq[2] = u32x4{dppu<NBC + 8>(pk), dppu<NBC + 9>(pk), dppu<NBC + 10>(pk), dppu<NBC + 11>(pk)};
      bq[3] = u32x4{dppu<NBC + 12>(pk), dppu<NBC + 13>(pk), dppu<NBC + 14>(pk), dppu<NBC + 15>(pk)};
      f32x4 a0 = zero4, a1 = zero4;
#pragma unroll
      for (int ks = 0; ks < 4; ++ks) { a0 = mfma32(auk[0][ks], bq[ks], a0); a1 = mfma32(auk[1][ks], bq[ks], a1); }
      const float sc = 0.08838834764831845f * 1.4426950408889634f;
      f32x4 a = (j & 1) ? a1 : a0;
      a *= sc;
      if (j < 2) st_dev(rs, WS_R2 + h * 2048 + (4 * cq + w) * 128 + (4 * j + q) * 16, granule(a, tag));
    }
    BAR();
    const int jq = cq, g = h >> 3;
#pragma unroll
    for (int n = 0; n < 4; ++n) {
      int row = 64 * w + 16 * n + (l >> 2);
      dma16(wuv, (u32)((row * 32 + h) * 256 + jq * 64 + (l & 3) * 16), lds0 + L_WUV + (64 * w + 16 * n) * 64);
    }
#pragma unroll
    for (int n = 0; n < 5; ++n)
      dma16(wo, (u32)((h * 128 + 32 * jq) * 1280 + (5 * w + n) * 1024 + l * 16), lds0 + L_WO + (5 * w + n) * 1024);
    if (w == 0) {
      u32 off[1] = {WS_FL + (u32)(g * 32 + (l & 31)) * 128}; bool act[1] = {true}; u32x4 v[1];
      poll_tagged<1>(rs, off, act, tag, v);
    }
    WAITVM(0);
    BAR();
    u32x4 buv[2][2];
    {
      const int rr = j >> 2, p = j & 3;
      const int c0 = 64 * w + 16 * (q >> 1) + 2 * (q & 1) + 32 * (rr & 1) + 4 * (rr >> 1);
#pragma unroll
      for (int dt = 0; dt < 2; ++dt)
#pragma unroll
        for (int a = 0; a < 2; ++a) {
          const u8* p0 = smem + L_WUV + (c0 + a) * 64 + dt * 32 + p * 8;
          buv[dt][a] = cat(trrd(p0), trrd(p0 + 8 * 64));
        }
    }
    u32x4 awo[3][2];
    {
      const int rr = j >> 2, p = j & 3;
#pragma unroll
      for (int pi = 0; pi < 3; ++pi) {
        int PP = w + 8 * pi; PP = PP < 20 ? PP : w;
#pragma unroll
        for (int tt = 0; tt < 2; ++tt) {
          const u8* p0 = smem + L_WO + (8 * q + rr) * 1280 + (32 * PP + 16 * tt + 4 * p) * 2;
          awo[pi][tt] = cat(trrd(p0), trrd(p0 + 4 * 1280));
        }
      }
    }
    {
      u32 off[1] = {WS_ML + (u32)(g * 32 + 4 * w + (l & 3)) * 128 + (h & 7) * 16}; bool act[1] = {true}; u32x4 v[1];
      poll_tagged<1>(rs, off, act, tag, v);
      int s = 4 * w + l;
      if (l < 4) *(u32x2*)(smem + L_MLB + (s & 7) * 32 + (s >> 3) * 8) = u32x2{v[0].x, v[0].z};
    }
    BAR();
    float e[4], inv;
    {
      const f32x4 m01 = *(const f32x4*)(smem + L_MLB + (l >> 3) * 32), m23 = *(const f32x4*)(smem + L_MLB + (l >> 3) * 32 + 16);
      float mx = fmaxf(fmaxf(m01[0], m01[2]), fmaxf(m23[0], m23[2]));
      mx = fmaxf(mx, dppf<ROR8>(mx)); mx = max16(mx); mx = max32(mx);
      e[0] = __builtin_amdgcn_exp2f(m01[0] - mx); e[1] = __builtin_amdgcn_exp2f(m01[2] - mx);
      e[2] = __builtin_amdgcn_exp2f(m23[0] - mx); e[3] = __builtin_amdgcn_exp2f(m23[2] - mx);
      float L = e[0] * m01[1] + e[1] * m01[3] + e[2] * m23[1] + e[3] * m23[3];
      L += dppf<ROR8>(L); L = red16(L, L); L = red32(L, L);
      inv = 1.0f / L;
    }
    u32x4 v3[4];
    {
      const u32 o = WS_R3 + (u32)(h * 32 + (l >> 3)) * 1024 + w * 128 + (l & 7) * 16;
      for (int spin = 0; spin < SPIN_MAX; ++spin) {
        bool bad = false;
        asm volatile("" ::: "memory");
#pragma unroll
        for (int i = 0; i < 4; ++i) v3[i] = ld_dev(rs, o + i * 8192);
#pragma unroll
        for (int i = 0; i < 4; ++i) bad |= (v3[i].x == SENT) | (v3[i].y == SENT) | (v3[i].z == SENT) | (v3[i].w == SENT);
        if (!wave_any(bad)) break;
      }
    }
    {
      float acc[8];
#pragma unroll
      for (int k = 0; k < 4; ++k) { acc[2 * k] = e[0] * bflo(v3[0][k]); acc[2 * k + 1] = e[0] * bfhi(v3[0][k]); }
#pragma unroll
      for (int i = 1; i < 4; ++i)
#pragma unroll
        for (int k = 0; k < 4; ++k) { acc[2 * k] += e[i] * bflo(v3[i][k]); acc[2 * k + 1] += e[i] * bfhi(v3[i][k]); }
      float r4[4], r2[2];
#pragma unroll
      for (int k = 0; k < 4; ++k) r4[k] = red32(acc[k], acc[k + 4]);
#pragma unroll
      for (int k = 0; k < 2; ++k) r2[k] = red16(r4[k], r4[k + 2]);
      const bool ah = (l & 8) != 0;
      float keep = ah ? r2[1] : r2[0], give = ah ? r2[0] : r2[1];
      float val = (keep + dppf<ROR8>(give)) * inv;
      const u32 pk = cvtpk(val, val);
      const u32 hm = (l & 1) ? 0xffff0000u : 0x0000ffffu;
      f32x4 d0 = zero4, d1 = zero4;
#pragma unroll
      for (int a = 0; a < 2; ++a) {
        u32x4 af;
#pragma unroll
        for (int k = 0; k < 4; ++k) af[k] = pk & ((((l & 7) >> 1) == k && (int)ah == a) ? hm : 0u);
        d0 = mfma32(af, buv[0][a], d0);
        d1 = mfma32(af, buv[1][a], d1);
      }
      float s0 = (d0[0] + d0[1]) + (d0[2] + d0[3]), s1 = (d1[0] + d1[1]) + (d1[2] + d1[3]);
      float s = red32(s0, s1);
      s = red16(s, s);
      if ((q & 1) == 0) *(float*)(smem + L_CTX + (16 * (l >> 5) + j) * 32 + w * 4) = s;
    }
    BAR();
    {
      const int d = 8 * q + (j & 7);
      const f32x4 c0 = *(const f32x4*)(smem + L_CTX + d * 32), c1 = *(const f32x4*)(smem + L_CTX + d * 32 + 16);
      const f32x4 cs = c0 + c1;
      const float v = (cs[0] + cs[1]) + (cs[2] + cs[3]);
      const u32 pk = cvtpk(v, dppf<XOR1>(v));
      const u32x4 bc = {dppu<NBC + 0>(pk), dppu<NBC + 2>(pk), dppu<NBC + 4>(pk), dppu<NBC + 6>(pk)};
      const int p = h * 4 + jq;
#pragma unroll
      for (int pi = 0; pi < 3; ++pi) {
        const int PP = w + 8 * pi;
        if (PP < 20) {
          f32x4 a0 = mfma32(awo[pi][0], bc, zero4), a1 = mfma32(awo[pi][1], bc, zero4);
          f32x4 a = (j & 1) ? a1 : a0;
          if (j < 2) st_dev(rs, WS_R5 + (u32)(PP * 128 + p) * 128 + (4 * j + q) * 16, granule(a, tag));
        }
      }
    }
  } else {
    const int g = idx & 3, s = 4 * xc + (idx >> 2);
    const u32 off3 = WS_R3 + (u32)((8 * g + (j & 7)) * 32 + s) * 1024 + w * 128 + 32 * q + 16 * (j >> 3);
    st_dev(rs, off3, u32x4{SENT, SENT, SENT, SENT});
    {
      const u8* base = (const u8*)ckv + (size_t)(128 * s + 16 * w) * 1024;
#pragma unroll
      for (int n = 0; n < 16; ++n) dma16(base + n * 1024, (u32)(l * 16), lds0 + L_CKV + (16 * w + n) * CKS);
    }
    const int Q = xc + 8 * (idx >> 2);
    WAITVM(0);
    BAR();
    const u32 tag = __builtin_amdgcn_readfirstlane(epoch_v) + 1u;
    if (w == 0 && l < 8) st_dev(rs, WS_FL + (u32)(g * 32 + s) * 128 + l * 16, u32x4{tag, tag, tag, tag});
    u32x4 aqk[16];
#pragma unroll
    for (int ks = 0; ks < 16; ++ks) aqk[ks] = *(const u32x4*)(smem + L_CKV + (16 * w + j) * CKS + ks * 64 + 16 * q);
    u32x4 apv[4][4];
    {
      const int rr = j >> 2, p = j & 3;
      const u8* p0 = smem + L_CKV + (4 * q + rr) * CKS + (64 * w + 4 * p) * 2;
#pragma unroll
      for (int kt = 0; kt < 4; ++kt)
#pragma unroll
        for (int tt = 0; tt < 4; ++tt)
          apv[kt][tt] = cat(trrd(p0 + (32 * kt) * CKS + tt * 32), trrd(p0 + (32 * kt + 16) * CKS + tt * 32));
    }
    {
      u32 off[2]; bool act[2] = {true, true}; u32x4 v[2];
#pragma unroll
      for (int i = 0; i < 2; ++i) off[i] = WS_R2 + (u32)(8 * g) * 2048 + (t + 512 * i) * 16;
      poll_tagged<2>(rs, off, act, tag, v);
#pragma unroll
      for (int i = 0; i < 2; ++i) {
        int n = t + 512 * i;
        *(u32x2*)(smem + L_QL + (n >> 7) * CKS + (n & 127) * 8) = u32x2{v[i].x, v[i].z};
      }
    }
    BAR();
    f32x4 sc;
    {
      f32x4 lo = zero4, hi = zero4;
      const u8* qb = smem + L_QL + (j & 7) * CKS + (j >> 3) * 512 + 16 * q;
#pragma unroll
      for (int u = 0; u < 8; ++u) {
        const u32x4 bq = *(const u32x4*)(qb + u * 64);
        lo = mfma32(aqk[u], bq, lo);
        hi = mfma32(aqk[u + 8], bq, hi);
      }
#pragma unroll
      for (int e = 0; e < 4; ++e) { float v = (j < 8) ? lo[e] : hi[e]; sc[e] = v + dppf<ROR8>(v); }
    }
    {
      float m = fmaxf(fmaxf(sc[0], sc[1]), fmaxf(sc[2], sc[3]));
      m = max16(m); m = max32(m);
      if (l < 8) *(float*)(smem + L_WM + l * 32 + w * 4) = m;
    }
    BAR();
    float mx;
    {
      const f32x4 a = *(const f32x4*)(smem + L_WM + (j & 7) * 32), c = *(const f32x4*)(smem + L_WM + (j & 7) * 32 + 16);
      mx = fmaxf(fmaxf(fmaxf(a[0], a[1]), fmaxf(a[2], a[3])), fmaxf(fmaxf(c[0], c[1]), fmaxf(c[2], c[3])));
      float p0 = __builtin_amdgcn_exp2f(sc[0] - mx), p1 = __builtin_amdgcn_exp2f(sc[1] - mx);
      float p2 = __builtin_amdgcn_exp2f(sc[2] - mx), p3 = __builtin_amdgcn_exp2f(sc[3] - mx);
      *(u32x2*)(smem + L_PF + (w >> 1) * 1024 + l * 16 + (w & 1) * 8) = u32x2{cvtpk(p0, p1), cvtpk(p2, p3)};
      float ls = (p0 + p1) + (p2 + p3);
      ls = red16(ls, ls); ls = red32(ls, ls);
      if (l < 8) *(float*)(smem + L_LP + l * 32 + w * 4) = ls;
    }
    BAR();
    {
      u32x4 pf[4];
#pragma unroll
      for (int kt = 0; kt < 4; ++kt) pf[kt] = *(const u32x4*)(smem + L_PF + kt * 1024 + l * 16);
      if (w == 0) {
        const f32x4 a = *(const f32x4*)(smem + L_LP + (j & 7) * 32), c = *(const f32x4*)(smem + L_LP + (j & 7) * 32 + 16);
        const f32x4 u = a + c;
        const float lt = (u[0] + u[1]) + (u[2] + u[3]);
        if (l < 8) st_dev(rs, WS_ML + (u32)(g * 32 + s) * 128 + l * 16, u32x4{__float_as_uint(mx), tag, __float_as_uint(lt), tag});
      }
      f32x4 o[4] = {zero4, zero4, zero4, zero4};
#pragma unroll
      for (int kt = 0; kt < 4; ++kt)
#pragma unroll
        for (int tt = 0; tt < 4; ++tt) o[tt] = mfma32(apv[kt][tt], pf[kt], o[tt]);
      const bool hb = j >= 8;
      const f32x4 oa = hb ? o[2] : o[0], ob = hb ? o[3] : o[1];
      u32x4 pv = {cvtpk(oa[0], oa[1]), cvtpk(oa[2], oa[3]), cvtpk(ob[0], ob[1]), cvtpk(ob[2], ob[3])};
#pragma unroll
      for (int k = 0; k < 4; ++k) pv[k] = pv[k] < 0xFFFFFFFEu ? pv[k] : 0xFFFFFFFEu;
      st_dev(rs, off3, pv);
    }
    if (Q < 27) {
      const int fg = 4 * Q + (idx & 3);
#pragma unroll
      for (int n = 0; n < 3; ++n) {
        const int it = w + 8 * n;
        if (it < 20) {
          const u32 vo = (u32)((32 * it + (l >> 1)) * 3456 + fg * 32 + (l & 1) * 16);
          dma16(wg, vo, lds0 + L_GATE + it * 1024);
          dma16(wu, vo, lds0 + L_UP + it * 1024);
          dma16(wd, (u32)(fg * 16 * 1280 + it * 1024 + l * 16), lds0 + L_DOWN + it * 1024);
        }
      }
      if (t < 320) *(u32*)(smem + L_ONES + t * 4) = 0x3F803F80u;
      WAITVM(0);
      BAR();
      const int rr = j >> 2, p = j & 3;
      u32x4 agu[2][5];
      if (w < 4) {
#pragma unroll
        for (int mat = 0; mat < 2; ++mat)
#pragma unroll
          for (int kk = 0; kk < 5; ++kk) {
            const u8* p0 = smem + (mat ? L_UP : L_GATE) + (32 * (5 * w + kk) + 8 * q + rr) * 32 + p * 8;
            agu[mat][kk] = cat(trrd(p0), trrd(p0 + 4 * 32));
          }
      }
      u32x2 adn[3][2];
#pragma unroll
      for (int pi = 0; pi < 3; ++pi) {
        int PP = w + 8 * pi; PP = PP < 20 ? PP : w;
#pragma unroll
        for (int tt = 0; tt < 2; ++tt) adn[pi][tt] = trrd(smem + L_DOWN + (4 * q + rr) * 1280 + (32 * PP + 16 * tt + 4 * p) * 2);
      }
      if (w < 3) {
        u32 off[1] = {WS_R6 + (u32)t * 16}; bool act[1] = {t < 160}; u32x4 v[1];
        poll_tagged<1>(rs, off, act, tag, v);
        if (t < 160) *(u32x2*)(smem + L_HB + t * 8) = u32x2{v[0].x, v[0].z};
      }
      BAR();
      if (w < 4) {
        f32x4 ga = zero4, ua = zero4;
        const u8* hbp = smem + ((j == 1) ? L_ONES : L_HB) + (160 * w + 8 * q) * 2;
#pragma unroll
        for (int kk = 0; kk < 5; ++kk) {
          const u32x4 bh = *(const u32x4*)(hbp + kk * 64);
          ga = mfma32(agu[0][kk], bh, ga);
          ua = mfma32(agu[1][kk], bh, ua);
        }
        if (j < 2) {
#pragma unroll
          for (int e = 0; e < 4; ++e) {
            *(float*)(smem + L_GP + ((0 + j) * 16 + 4 * q + e) * 16 + w * 4) = ga[e];
            *(float*)(smem + L_GP + ((2 + j) * 16 + 4 * q + e) * 16 + w * 4) = ua[e];
          }
        }
      } else {
        const float K = bflo(*(const u16*)(smem + L_HB));
        const u32x2 hv = *(const u32x2*)(smem + L_HB + (160 * (w - 4) + 4 * (l < 40 ? l : 0)) * 2);
        float d0 = bflo(hv.x) - K, d1 = bfhi(hv.x) - K, d2 = bflo(hv.y) - K, d3 = bfhi(hv.y) - K;
        float s1 = (d0 + d1) + (d2 + d3), s2 = (d0 * d0 + d1 * d1) + (d2 * d2 + d3 * d3);
        if (l >= 40) { s1 = 0.f; s2 = 0.f; }
        s1 = wave_sum(s1); s2 = wave_sum(s2);
        if (l == 0) {
          *(f32x2*)(smem + L_ST + (w - 4) * 8) = f32x2{s1, s2};
          if (w == 4) *(float*)(smem + L_ST + 32) = K;
        }
      }
      BAR();
      {
        const int mat = (j >> 2) & 1, f = 4 * q + (j & 3);
        const f32x4 hw = *(const f32x4*)(smem + L_GP + ((2 * mat + 0) * 16 + f) * 16);
        const f32x4 cs = *(const f32x4*)(smem + L_GP + ((2 * mat + 1) * 16 + f) * 16);
        const f32x4 s01 = *(const f32x4*)(smem + L_ST), s23 = *(const f32x4*)(smem + L_ST + 16);
        const float K = *(const float*)(smem + L_ST + 32);
        const float m1 = ((s01[0] + s01[2]) + (s23[0] + s23[2])) * (1.0f / 640.0f);
        const float m2 = ((s01[1] + s01[3]) + (s23[1] + s23[3])) * (1.0f / 640.0f);
        const float mean = K + m1;
        const float rstd = 1.0f / sqrtf(fmaxf(m2 - m1 * m1, 0.f) + 1e-5f);
        const float val = rstd * (((hw[0] + hw[1]) + (hw[2] + hw[3])) - mean * ((cs[0] + cs[1]) + (cs[2] + cs[3])));
        const float up = dppf<SHL4>(val);
        const float hh = (val > 0.f ? val : 0.f) * up;
        const u32 pk = cvtpk(hh, dppf<XOR1>(hh));
        const u32x2 bd = {dppu<NBC + 0>(pk), dppu<NBC + 2>(pk)};
#pragma unroll
        for (int pi = 0; pi < 3; ++pi) {
          const int PP = w + 8 * pi;
          if (PP < 20) {
            f32x4 a0 = mfma16(adn[pi][0], bd, zero4), a1 = mfma16(adn[pi][1], bd, zero4);
            f32x4 a = (j & 1) ? a1 : a0;
            if (j < 2) st_dev(rs, WS_R7 + (u32)(PP * 108 + fg) * 128 + (4 * j + q) * 16, granule(a, tag));
          }
        }
      }
    } else {
      const int cg = 4 * (Q - 27) + (idx & 3);
      const u32x2 xv = *(const u32x2*)((const u8*)x + (32 * cg + 4 * (l & 7)) * 2);
      f32x4 hsum = reduce_lines<128>(rs, WS_R5 + (u32)cg * 128 * 128, tag, smem + L_RED5, t, w, l);
      f32x4 hv = {bflo(xv.x) + hsum[0], bfhi(xv.x) + hsum[1], bflo(xv.y) + hsum[2], bfhi(xv.y) + hsum[3]};
      if (w == 0 && l < 8) st_dev(rs, WS_R6 + (u32)cg * 128 + l * 16, granule(hv, tag));
      f32x4 fsum = reduce_lines<108>(rs, WS_R7 + (u32)cg * 108 * 128, tag, smem + L_RED7, t, w, l);
      if (w == 0 && l < 8) {
        hv += fsum;
        *(u32x2*)((u8*)out + (32 * cg + 4 * l) * 2) = u32x2{cvtpk(hv[0], hv[1]), cvtpk(hv[2], hv[3])};
        if (cg == 0 && l == 0) __builtin_amdgcn_raw_buffer_store_b32(tag, rs, 0, 0, 16);
      }
    }
  }
}

extern "C" void kernel_launch(void* const* d_in, const int* in_sizes, int n_in, void* d_out, int out_size, void* d_ws,
                              size_t ws_size, hipStream_t stream) {
  (void)in_sizes; (void)n_in; (void)out_size; (void)ws_size;
  mla_ffn_decode_kernel<<<256, 512, 0, stream>>>((const u16*)d_in[0], (const u16*)d_in[1], (const u16*)d_in[2],
                                                 (const u16*)d_in[3], (const u16*)d_in[4], (const u16*)d_in[5],
                                                 (const u16*)d_in[7], (const u16*)d_in[8], (const u16*)d_in[9],
                                                 (u16*)d_out, (u8*)d_ws);
}
```

```cpp
#include <hip/hip_runtime.h>
#include <hip/hip_bf16.h>

typedef unsigned int u32;
typedef unsigned short u16;
typedef unsigned char u8;
typedef float f32x4 __attribute__((ext_vector_type(4)));
typedef float f32x2 __attribute__((ext_vector_type(2)));
typedef u32 u32x4 __attribute__((ext_vector_type(4)));
typedef u32 u32x2 __attribute__((ext_vector_type(2)));
typedef __bf16 bf16x8 __attribute__((ext_vector_type(8)));
typedef __bf16 bf16x2 __attribute__((ext_vector_type(2)));
typedef short s16x4 __attribute__((ext_vector_type(4)));
typedef __amdgpu_buffer_rsrc_t rsrc_t;

#define DEV __device__ __forceinline__

constexpr u32 WS_R2 = 4096;
constexpr u32 WS_FL = WS_R2 + 65536;
constexpr u32 WS_ML = WS_FL + 16384;
constexpr u32 WS_R6 = WS_ML + 16384;
constexpr u32 WS_R5 = WS_R6 + 4096;
constexpr u32 WS_R7 = WS_R5 + 20 * 128 * 128;
constexpr u32 WS_R3 = WS_R7 + 20 * 108 * 128 + 2048;
constexpr u32 WS_END = WS_R3 + 32 * 32 * 1024;

constexpr u32 L_WUK = 0, L_WUV = 32768, L_WO = 65536, L_QP = 106496, L_MLB = 114688, L_CTX = 114944;
constexpr u32 CKS = 1056;
constexpr u32 L_CKV = 0, L_QL = 128 * CKS, L_WM = L_QL + 8 * CKS, L_LP = L_WM + 256, L_PF = L_LP + 256;
constexpr u32 LDS_BYTES = L_PF + 4096;
constexpr u32 L_GATE = 0, L_UP = 20480, L_DOWN = 40960, L_ONES = 61440, L_HB = 62720, L_GP = 64000, L_ST = 65024;
constexpr u32 L_RED5 = 0, L_RED7 = 1024;

constexpr int PF_LINES = 126;
constexpr int SPIN_MAX = 1 << 14;
constexpr u32 SENT = 0xFFFFFFFFu;

DEV f32x4 mfma32(u32x4 a, u32x4 b, f32x4 c) {
  return __builtin_amdgcn_mfma_f32_16x16x32_bf16(__builtin_bit_cast(bf16x8, a), __builtin_bit_cast(bf16x8, b), c, 0, 0, 0);
}
DEV f32x4 mfma16(u32x2 a, u32x2 b, f32x4 c) {
  return __builtin_amdgcn_mfma_f32_16x16x16bf16_1k(__builtin_bit_cast(s16x4, a), __builtin_bit_cast(s16x4, b), c, 0, 0, 0);
}
DEV u32 cvtpk(float lo, float hi) {
  f32x2 v = {lo, hi};
  return __builtin_bit_cast(u32, __builtin_convertvector(v, bf16x2));
}
DEV float bflo(u32 u) { return __uint_as_float(u << 16); }
DEV float bfhi(u32 u) { return __uint_as_float(u & 0xffff0000u); }
template <int CTRL> DEV u32 dppu(u32 v) { return (u32)__builtin_amdgcn_update_dpp(0, (int)v, CTRL, 0xf, 0xf, true); }
template <int CTRL> DEV float dppf(float v) { return __uint_as_float(dppu<CTRL>(__float_as_uint(v))); }
constexpr int ROR8 = 0x128, ROR4 = 0x124, ROR2 = 0x122, ROR1 = 0x121, XOR1 = 0xB1, SHL4 = 0x104, NBC = 0x150;
DEV float red32(float a, float b) {
  auto s = __builtin_amdgcn_permlane32_swap(__float_as_uint(a), __float_as_uint(b), false, false);
  return __uint_as_float(s[0]) + __uint_as_float(s[1]);
}
DEV float red16(float a, float b) {
  auto s = __builtin_amdgcn_permlane16_swap(__float_as_uint(a), __float_as_uint(b), false, false);
  return __uint_as_float(s[0]) + __uint_as_float(s[1]);
}
DEV float max32(float a) {
  auto s = __builtin_amdgcn_permlane32_swap(__float_as_uint(a), __float_as_uint(a), false, false);
  return fmaxf(__uint_as_float(s[0]), __uint_as_float(s[1]));
}
DEV float max16(float a) {
  auto s = __builtin_amdgcn_permlane16_swap(__float_as_uint(a), __float_as_uint(a), false, false);
  return fmaxf(__uint_as_float(s[0]), __uint_as_float(s[1]));
}
DEV float wave_sum(float v) {
  v += dppf<ROR8>(v); v += dppf<ROR4>(v); v += dppf<ROR2>(v); v += dppf<ROR1>(v);
  v = red16(v, v);
  return red32(v, v);
}
DEV u32x2 trrd(const u8* p) {
  typedef short v4i16_t __attribute__((ext_vector_type(4)));
  return __builtin_bit_cast(u32x2, __builtin_amdgcn_ds_read_tr16_b64_v4i16((__attribute__((address_space(3))) v4i16_t*)p));
}
DEV u32x4 cat(u32x2 a, u32x2 b) { return u32x4{a.x, a.y, b.x, b.y}; }
DEV void dma16(const void* sbase, u32 voff, u32 lds) {
  u32 keep;
  asm volatile("s_mov_b32 %0, m0\n\ts_mov_b32 m0, %3\n\ts_nop 0\n\tglobal_load_lds_dwordx4 %1, %2\n\ts_mov_b32 m0, %0"
               : "=&s"(keep) : "v"(voff), "s"(sbase), "s"(lds) : "memory");
}
#define BAR() asm volatile("s_waitcnt lgkmcnt(0)\n\ts_barrier" ::: "memory")
#define WAITVM(n) asm volatile("s_waitcnt vmcnt(" #n ")" ::: "memory")
DEV u32x4 ld_dev(rsrc_t rs, u32 off) { return __builtin_amdgcn_raw_buffer_load_b128(rs, off, 0, 16); }
DEV void st_dev(rsrc_t rs, u32 off, u32x4 v) { __builtin_amdgcn_raw_buffer_store_b128(v, rs, off, 0, 16); }
DEV bool wave_any(bool p) { return __builtin_amdgcn_ballot_w64(p) != 0; }

template <int N> DEV void poll_tagged(rsrc_t rs, const u32 (&off)[N], const bool (&act)[N], u32 tag, u32x4 (&v)[N]) {
  for (int spin = 0; spin < SPIN_MAX; ++spin) {
    bool bad = false;
    asm volatile("" ::: "memory");
#pragma unroll
    for (int i = 0; i < N; ++i) {
      v[i] = u32x4{0, tag, 0, tag};
      if (act[i]) v[i] = ld_dev(rs, off[i]);
    }
#pragma unroll
    for (int i = 0; i < N; ++i) bad |= (v[i].y != tag) | (v[i].w != tag);
    if (!wave_any(bad)) break;
  }
}
DEV u32x4 granule(f32x4 a, u32 tag) { return u32x4{cvtpk(a[0], a[1]), tag, cvtpk(a[2], a[3]), tag}; }

template <int NP> DEV f32x4 reduce_lines(rsrc_t rs, u32 base, u32 tag, u8* red, int t, int w, int l) {
  u32 off[2]; bool act[2]; u32x4 v[2];
#pragma unroll
  for (int i = 0; i < 2; ++i) {
    int p = (t >> 3) + 64 * i;
    act[i] = p < NP;
    off[i] = base + p * 128 + (t & 7) * 16;
  }
  poll_tagged<2>(rs, off, act, tag, v);
  float a0 = bflo(v[0].x) + bflo(v[1].x), a1 = bfhi(v[0].x) + bfhi(v[1].x);
  float a2 = bflo(v[0].z) + bflo(v[1].z), a3 = bfhi(v[0].z) + bfhi(v[1].z);
  float r0 = red32(a0, a2), r1 = red32(a1, a3);
  float r = red16(r0, r1);
  r += dppf<ROR8>(r);
  if ((l & 8) == 0) *(float*)(red + w * 128 + (4 * (l & 7) + (l >> 4)) * 4) = r;
  BAR();
  f32x4 s = {0.f, 0.f, 0.f, 0.f};
  if (w == 0) {
#pragma unroll
    for (int i = 0; i < 8; ++i) s += *(const f32x4*)(red + i * 128 + (l & 7) * 16);
  }
  return s;
}

__global__ void __launch_bounds__(512, 2)
mla_ffn_decode_kernel(const u16* __restrict__ x, const u16* __restrict__ ckv, const u16* __restrict__ wq,
                      const u16* __restrict__ wuk, const u16* __restrict__ wuv, const u16* __restrict__ wo,
                      const u16* __restrict__ wg, const u16* __restrict__ wu, const u16* __restrict__ wd,
                      u16* __restrict__ out, u8* __restrict__ ws) {
  __shared__ __attribute__((aligned(16))) u8 smem[LDS_BYTES + 256];
  const u32 lds0 = (u32)(size_t)(__attribute__((address_space(3))) u8*)smem;
  const int b = blockIdx.x, xc = b & 7, r = b >> 3, idx = r & 15;
  const int t = threadIdx.x, l = t & 63, q = l >> 4, j = l & 15;
  const int w = __builtin_amdgcn_readfirstlane(t >> 6);
  const rsrc_t rs = __builtin_amdgcn_make_buffer_rsrc((void*)ws, 0, WS_END, 0x00020000);
  const u32 epoch_v = __builtin_amdgcn_raw_buffer_load_b32(rs, 0, 0, 16);
  const f32x4 zero4 = {0.f, 0.f, 0.f, 0.f};
  if (w == 6) {
    const int pg = r * 8 + (l & 7);
    const u8* bp; u32 po;
    if (pg < 64) { bp = (const u8*)wuv; po = pg; }
    else if (pg < 144) { bp = (const u8*)wo; po = pg - 64; }
    else if (pg < 177) { bp = (const u8*)wg; po = pg - 144; }
    else if (pg < 210) { bp = (const u8*)wu; po = pg - 177; }
    else if (pg < 243) { bp = (const u8*)wd; po = pg - 210; }
    else { bp = (const u8*)out; po = 0; }
    const u8* ap = bp + (size_t)po * 65536;
    u32 keep;
    if (l < 8)
      asm volatile("s_mov_b32 %0, m0\n\ts_mov_b32 m0, %2\n\ts_nop 0\n\tglobal_load_lds_dword %1, off\n\ts_mov_b32 m0, %0"
               : "=&s"(keep) : "v"(ap), "s"(lds0 + LDS_BYTES) : "memory");
  }
  if (w == 7) {
    u32 pf_lo, pf_hi;
    asm volatile("s_getpc_b64 s[20:21]\n\ts_mov_b32 %0, s20\n\ts_mov_b32 %1, s21" : "=s"(pf_lo), "=s"(pf_hi) : : "s20", "s21");
    const u8* pc = (const u8*)(((unsigned long long)pf_hi << 32) | (pf_lo & ~127u));
    u32 keep;
    if (l < 4 && r * 4 + l < PF_LINES)
      asm volatile("s_mov_b32 %0, m0\n\ts_mov_b32 m0, %3\n\ts_nop 0\n\tglobal_load_lds_dword %1, %2\n\ts_mov_b32 m0, %0"
               : "=&s"(keep) : "v"((u32)((r * 4 + l) * 128)), "s"(pc), "s"(lds0 + LDS_BYTES) : "memory");
  }

  if (r < 16) {
    const int h = 4 * xc + (idx >> 2), cq = idx & 3;
    if (w < 4) {
#pragma unroll
      for (int n = 0; n < 8; ++n) {
        int row = 32 * w + 4 * n + q;
        dma16(wuk, (u32)(((128 * cq + row) * 32 + h) * 256 + ((j ^ (row & 15)) * 16)), lds0 + L_WUK + (32 * w + 4 * n) * 256);
      }
    }
    const u32x4 xa = *(const u32x4*)((const u8*)x + l * 16);
    const u32 xb = *(const u32*)((const u8*)x + 1024 + l * 4);
    u32x2 xr[5];
#pragma unroll
    for (int k = 0; k < 5; ++k) xr[k] = *(const u32x2*)((const u8*)x + (k * 128 + (4 * w + q) * 4) * 2);
    u32x4 wv[20];
    {
      const u8* base = (const u8*)wq + h * 256;
      const u32 vo = (u32)((4 * w + q) * 4 * 8192 + j * 16);
#pragma unroll
      for (int i = 0; i < 20; ++i) wv[i] = *(const u32x4*)(base + ((i >> 2) * 128 + (i & 3)) * 8192 + vo);
    }
    __builtin_amdgcn_sched_barrier(0);
    WAITVM(27);
    u32x4 auk[2][4];
    if (w < 4) {
#pragma unroll
      for (int tt = 0; tt < 2; ++tt)
#pragma unroll
        for (int ks = 0; ks < 4; ++ks)
          auk[tt][ks] = *(const u32x4*)(smem + L_WUK + (32 * w + 16 * tt + j) * 256 + (((4 * ks + q) ^ j) * 16));
    }
    float mean, rstd;
    {
      float v[10] = {bflo(xa.x), bfhi(xa.x), bflo(xa.y), bfhi(xa.y), bflo(xa.z), bfhi(xa.z), bflo(xa.w), bfhi(xa.w), bflo(xb), bfhi(xb)};
      float s = 0.f;
#pragma unroll
      for (int i = 0; i < 10; ++i) s += v[i];
      mean = wave_sum(s) * (1.0f / 640.0f);
      float s2 = 0.f;
#pragma unroll
      for (int i = 0; i < 10; ++i) { float d = v[i] - mean; s2 += d * d; }
      rstd = 1.0f / sqrtf(wave_sum(s2) * (1.0f / 640.0f) + 1e-5f);
    }
    {
      u32 sel[4][2];
#pragma unroll
      for (int k = 0; k < 4; ++k) {
        bool own = ((j & 7) >> 1) == k;
        sel[k][0] = own ? ((j & 1) ? 0x01000c0cu : 0x0c0c0100u) : 0x0c0c0c0cu;
        sel[k][1] = own ? ((j & 1) ? 0x03020c0cu : 0x0c0c0302u) : 0x0c0c0c0cu;
      }
      f32x4 acc0 = zero4, acc1 = zero4;
#pragma unroll
      for (int i = 0; i < 20; ++i) {
        u32 xw = (i & 2) ? xr[i >> 2].y : xr[i >> 2].x;
        xw = (j < 8) ? xw : 0x3F803F80u;
        u32x4 bf;
#pragma unroll
        for (int k = 0; k < 4; ++k) bf[k] = __builtin_amdgcn_perm(xw, xw, sel[k][i & 1]);
        if (i & 1) acc1 = mfma32(wv[i], bf, acc1); else acc0 = mfma32(wv[i], bf, acc0);
      }
      acc0 += acc1;
#pragma unroll
      for (int e = 0; e < 4; ++e) {
        int P = 16 * q + 4 * e + ((j & 7) >> 1);
        int pos = P ^ ((q & 1) << 3);
        *(float*)(smem + L_QP + w * 1024 + pos * 16 + (j >> 3) * 8 + (j & 1) * 4) = acc0[e];
      }
    }
    BAR();
    const u32 tag = __builtin_amdgcn_readfirstlane(epoch_v) + 1u;
    if (w < 4) {
      const int P = 16 * (j >> 2) + 4 * q + (j & 3);
      const int pos = P ^ (((j >> 2) & 1) << 3);
      f32x4 s = zero4;
#pragma unroll
      for (int i = 0; i < 8; ++i) s += *(const f32x4*)(smem + L_QP + i * 1024 + pos * 16);
      const u32 pk = cvtpk(rstd * (s[0] - mean * s[2]), rstd * (s[1] - mean * s[3]));
      u32x4 bq[4];
      bq[0] = u32x4{dppu<NBC + 0>(pk), dppu<NBC + 1>(pk), dppu<NBC + 2>(pk), dppu<NBC + 3>(pk)};
      bq[1] = u32x4{dppu<NBC + 4>(pk), dppu<NBC + 5>(pk), dppu<NBC + 6>(pk), dppu<NBC + 7>(pk)};
      bq[2] = u32x4{dppu<NBC + 8>(pk), dppu<NBC + 9>(pk), dppu<NBC + 10>(pk), dppu<NBC + 11>(pk)};
      bq[3] = u32x4{dppu<NBC + 12>(pk), dppu<NBC + 13>(pk), dppu<NBC + 14>(pk), dppu<NBC + 15>(pk)};
      f32x4 a0 = zero4, a1 = zero4;
#pragma unroll
      for (int ks = 0; ks < 4; ++ks) { a0 = mfma32(auk[0][ks], bq[ks], a0); a1 = mfma32(auk[1][ks], bq[ks], a1); }
      const float sc = 0.08838834764831845f * 1.4426950408889634f;
      f32x4 a = (j & 1) ? a1 : a0;
      a *= sc;
      if (j < 2) st_dev(rs, WS_R2 + h * 2048 + (4 * cq + w) * 128 + (4 * j + q) * 16, granule(a, tag));
    }
    BAR();
    const int jq = cq, g = h >> 3;
#pragma unroll
    for (int n = 0; n < 4; ++n) {
      int row = 64 * w + 16 * n + (l >> 2);
      dma16(wuv, (u32)((row * 32 + h) * 256 + jq * 64 + (l & 3) * 16), lds0 + L_WUV + (64 * w + 16 * n) * 64);
    }
#pragma unroll
    for (int n = 0; n < 5; ++n)
      dma16(wo, (u32)((h * 128 + 32 * jq) * 1280 + (5 * w + n) * 1024 + l * 16), lds0 + L_WO + (5 * w + n) * 1024);
    if (w == 0) {
      u32 off[1] = {WS_FL + (u32)(g * 32 + (l & 31)) * 128}; bool act[1] = {true}; u32x4 v[1];
      poll_tagged<1>(rs, off, act, tag, v);
    }
    WAITVM(0);
    BAR();
    u32x4 buv[2][2];
    {
      const int rr = j >> 2, p = j & 3;
      const int c0 = 64 * w + 16 * (q >> 1) + 2 * (q & 1) + 32 * (rr & 1) + 4 * (rr >> 1);
#pragma unroll
      for (int dt = 0; dt < 2; ++dt)
#pragma unroll
        for (int a = 0; a < 2; ++a) {
          const u8* p0 = smem + L_WUV + (c0 + a) * 64 + dt * 32 + p * 8;
          buv[dt][a] = cat(trrd(p0), trrd(p0 + 8 * 64));
        }
    }
    u32x4 awo[3][2];
    {
      const int rr = j >> 2, p = j & 3;
#pragma unroll
      for (int pi = 0; pi < 3; ++pi) {
        int PP = w + 8 * pi; PP = PP < 20 ? PP : w;
#pragma unroll
        for (int tt = 0; tt < 2; ++tt) {
          const u8* p0 = smem + L_WO + (8 * q + rr) * 1280 + (32 * PP + 16 * tt + 4 * p) * 2;
          awo[pi][tt] = cat(trrd(p0), trrd(p0 + 4 * 1280));
        }
      }
    }
    {
      u32 off[1] = {WS_ML + (u32)(g * 32 + 4 * w + (l & 3)) * 128 + (h & 7) * 16}; bool act[1] = {true}; u32x4 v[1];
      poll_tagged<1>(rs, off, act, tag, v);
      int s = 4 * w + l;
      if (l < 4) *(u32x2*)(smem + L_MLB + (s & 7) * 32 + (s >> 3) * 8) = u32x2{v[0].x, v[0].z};
    }
    BAR();
    float e[4], inv;
    {
      const f32x4 m01 = *(const f32x4*)(smem + L_MLB + (l >> 3) * 32), m23 = *(const f32x4*)(smem + L_MLB + (l >> 3) * 32 + 16);
      float mx = fmaxf(fmaxf(m01[0], m01[2]), fmaxf(m23[0], m23[2]));
      mx = fmaxf(mx, dppf<ROR8>(mx)); mx = max16(mx); mx = max32(mx);
      e[0] = __builtin_amdgcn_exp2f(m01[0] - mx); e[1] = __builtin_amdgcn_exp2f(m01[2] - mx);
      e[2] = __builtin_amdgcn_exp2f(m23[0] - mx); e[3] = __builtin_amdgcn_exp2f(m23[2] - mx);
      float L = e[0] * m01[1] + e[1] * m01[3] + e[2] * m23[1] + e[3] * m23[3];
      L += dppf<ROR8>(L); L = red16(L, L); L = red32(L, L);
      inv = 1.0f / L;
    }
    u32x4 v3[4];
    {
      const u32 o = WS_R3 + (u32)(h * 32 + (l >> 3)) * 1024 + w * 128 + (l & 7) * 16;
      for (int spin = 0; spin < SPIN_MAX; ++spin) {
        bool bad = false;
        asm volatile("" ::: "memory");
#pragma unroll
        for (int i = 0; i < 4; ++i) v3[i] = ld_dev(rs, o + i * 8192);
#pragma unroll
        for (int i = 0; i < 4; ++i) bad |= (v3[i].x == SENT) | (v3[i].y == SENT) | (v3[i].z == SENT) | (v3[i].w == SENT);
        if (!wave_any(bad)) break;
      }
    }
    {
      float acc[8];
#pragma unroll
      for (int k = 0; k < 4; ++k) { acc[2 * k] = e[0] * bflo(v3[0][k]); acc[2 * k + 1] = e[0] * bfhi(v3[0][k]); }
#pragma unroll
      for (int i = 1; i < 4; ++i)
#pragma unroll
        for (int k = 0; k < 4; ++k) { acc[2 * k] += e[i] * bflo(v3[i][k]); acc[2 * k + 1] += e[i] * bfhi(v3[i][k]); }
      float r4[4], r2[2];
#pragma unroll
      for (int k = 0; k < 4; ++k) r4[k] = red32(acc[k], acc[k + 4]);
#pragma unroll
      for (int k = 0; k < 2; ++k) r2[k] = red16(r4[k], r4[k + 2]);
      const bool ah = (l & 8) != 0;
      float keep = ah ? r2[1] : r2[0], give = ah ? r2[0] : r2[1];
      float val = (keep + dppf<ROR8>(give)) * inv;
      const u32 pk = cvtpk(val, val);
      const u32 hm = (l & 1) ? 0xffff0000u : 0x0000ffffu;
      f32x4 d0 = zero4, d1 = zero4;
#pragma unroll
      for (int a = 0; a < 2; ++a) {
        u32x4 af;
#pragma unroll
        for (int k = 0; k < 4; ++k) af[k] = pk & ((((l & 7) >> 1) == k && (int)ah == a) ? hm : 0u);
        d0 = mfma32(af, buv[0][a], d0);
        d1 = mfma32(af, buv[1][a], d1);
      }
      float s0 = (d0[0] + d0[1]) + (d0[2] + d0[3]), s1 = (d1[0] + d1[1]) + (d1[2] + d1[3]);
      float s = red32(s0, s1);
      s = red16(s, s);
      if ((q & 1) == 0) *(float*)(smem + L_CTX + (16 * (l >> 5) + j) * 32 + w * 4) = s;
    }
    BAR();
    {
      const int d = 8 * q + (j & 7);
      const f32x4 c0 = *(const f32x4*)(smem + L_CTX + d * 32), c1 = *(const f32x4*)(smem + L_CTX + d * 32 + 16);
      const f32x4 cs = c0 + c1;
      const float v = (cs[0] + cs[1]) + (cs[2] + cs[3]);
      const u32 pk = cvtpk(v, dppf<XOR1>(v));
      const u32x4 bc = {dppu<NBC + 0>(pk), dppu<NBC + 2>(pk), dppu<NBC + 4>(pk), dppu<NBC + 6>(pk)};
      const int p = h * 4 + jq;
#pragma unroll
      for (int pi = 0; pi < 3; ++pi) {
        const int PP = w + 8 * pi;
        if (PP < 20) {
          f32x4 a0 = mfma32(awo[pi][0], bc, zero4), a1 = mfma32(awo[pi][1], bc, zero4);
          f32x4 a = (j & 1) ? a1 : a0;
          if (j < 2) st_dev(rs, WS_R5 + (u32)(PP * 128 + p) * 128 + (4 * j + q) * 16, granule(a, tag));
        }
      }
    }
  } else {
    const int g = idx & 3, s = 4 * xc + (idx >> 2);
    const u32 off3 = WS_R3 + (u32)((8 * g + (j & 7)) * 32 + s) * 1024 + w * 128 + 32 * q + 16 * (j >> 3);
    st_dev(rs, off3, u32x4{SENT, SENT, SENT, SENT});
    {
      const u8* base = (const u8*)ckv + (size_t)(128 * s + 16 * w) * 1024;
#pragma unroll
      for (int n = 0; n < 16; ++n) dma16(base + n * 1024, (u32)(l * 16), lds0 + L_CKV + (16 * w + n) * CKS);
    }
    const int Q = xc + 8 * (idx >> 2);
    WAITVM(0);
    BAR();
    const u32 tag = __builtin_amdgcn_readfirstlane(epoch_v) + 1u;
    if (w == 0 && l < 8) st_dev(rs, WS_FL + (u32)(g * 32 + s) * 128 + l * 16, u32x4{tag, tag, tag, tag});
    u32x4 aqk[16];
#pragma unroll
    for (int ks = 0; ks < 16; ++ks) aqk[ks] = *(const u32x4*)(smem + L_CKV + (16 * w + j) * CKS + ks * 64 + 16 * q);
    u32x4 apv[4][4];
    {
      const int rr = j >> 2, p = j & 3;
      const u8* p0 = smem + L_CKV + (4 * q + rr) * CKS + (64 * w + 4 * p) * 2;
#pragma unroll
      for (int kt = 0; kt < 4; ++kt)
#pragma unroll
        for (int tt = 0; tt < 4; ++tt)
          apv[kt][tt] = cat(trrd(p0 + (32 * kt) * CKS + tt * 32), trrd(p0 + (32 * kt + 16) * CKS + tt * 32));
    }
    {
      u32 off[2]; bool act[2] = {true, true}; u32x4 v[2];
#pragma unroll
      for (int i = 0; i < 2; ++i) off[i] = WS_R2 + (u32)(8 * g) * 2048 + (t + 512 * i) * 16;
      poll_tagged<2>(rs, off, act, tag, v);
#pragma unroll
      for (int i = 0; i < 2; ++i) {
        int n = t + 512 * i;
        *(u32x2*)(smem + L_QL + (n >> 7) * CKS + (n & 127) * 8) = u32x2{v[i].x, v[i].z};
      }
    }
    BAR();
    f32x4 sc;
    {
      f32x4 lo = zero4, hi = zero4;
      const u8* qb = smem + L_QL + (j & 7) * CKS + (j >> 3) * 512 + 16 * q;
#pragma unroll
      for (int u = 0; u < 8; ++u) {
        const u32x4 bq = *(const u32x4*)(qb + u * 64);
        lo = mfma32(aqk[u], bq, lo);
        hi = mfma32(aqk[u + 8], bq, hi);
      }
#pragma unroll
      for (int e = 0; e < 4; ++e) { float v = (j < 8) ? lo[e] : hi[e]; sc[e] = v + dppf<ROR8>(v); }
    }
    {
      float m = fmaxf(fmaxf(sc[0], sc[1]), fmaxf(sc[2], sc[3]));
      m = max16(m); m = max32(m);
      if (l < 8) *(float*)(smem + L_WM + l * 32 + w * 4) = m;
    }
    BAR();
    float mx;
    {
      const f32x4 a = *(const f32x4*)(smem + L_WM + (j & 7) * 32), c = *(const f32x4*)(smem + L_WM + (j & 7) * 32 + 16);
      mx = fmaxf(fmaxf(fmaxf(a[0], a[1]), fmaxf(a[2], a[3])), fmaxf(fmaxf(c[0], c[1]), fmaxf(c[2], c[3])));
      float p0 = __builtin_amdgcn_exp2f(sc[0] - mx), p1 = __builtin_amdgcn_exp2f(sc[1] - mx);
      float p2 = __builtin_amdgcn_exp2f(sc[2] - mx), p3 = __builtin_amdgcn_exp2f(sc[3] - mx);
      *(u32x2*)(smem + L_PF + (w >> 1) * 1024 + l * 16 + (w & 1) * 8) = u32x2{cvtpk(p0, p1), cvtpk(p2, p3)};
      float ls = (p0 + p1) + (p2 + p3);
      ls = red16(ls, ls); ls = red32(ls, ls);
      if (l < 8) *(float*)(smem + L_LP + l * 32 + w * 4) = ls;
    }
    BAR();
    {
      u32x4 pf[4];
#pragma unroll
      for (int kt = 0; kt < 4; ++kt) pf[kt] = *(const u32x4*)(smem + L_PF + kt * 1024 + l * 16);
      if (w == 0) {
        const f32x4 a = *(const f32x4*)(smem + L_LP + (j & 7) * 32), c = *(const f32x4*)(smem + L_LP + (j & 7) * 32 + 16);
        const f32x4 u = a + c;
        const float lt = (u[0] + u[1]) + (u[2] + u[3]);
        if (l < 8) st_dev(rs, WS_ML + (u32)(g * 32 + s) * 128 + l * 16, u32x4{__float_as_uint(mx), tag, __float_as_uint(lt), tag});
      }
      f32x4 o[4] = {zero4, zero4, zero4, zero4};
#pragma unroll
      for (int kt = 0; kt < 4; ++kt)
#pragma unroll
        for (int tt = 0; tt < 4; ++tt) o[tt] = mfma32(apv[kt][tt], pf[kt], o[tt]);
      const bool hb = j >= 8;
      const f32x4 oa = hb ? o[2] : o[0], ob = hb ? o[3] : o[1];
      u32x4 pv = {cvtpk(oa[0], oa[1]), cvtpk(oa[2], oa[3]), cvtpk(ob[0], ob[1]), cvtpk(ob[2], ob[3])};
#pragma unroll
      for (int k = 0; k < 4; ++k) pv[k] = pv[k] < 0xFFFFFFFEu ? pv[k] : 0xFFFFFFFEu;
      st_dev(rs, off3, pv);
    }
    if (Q < 27) {
      const int fg = 4 * Q + (idx & 3);
#pragma unroll
      for (int n = 0; n < 3; ++n) {
        const int it = w + 8 * n;
        if (it < 20) {
          const u32 vo = (u32)((32 * it + (l >> 1)) * 3456 + fg * 32 + (l & 1) * 16);
          dma16(wg, vo, lds0 + L_GATE + it * 1024);
          dma16(wu, vo, lds0 + L_UP + it * 1024);
          dma16(wd, (u32)(fg * 16 * 1280 + it * 1024 + l * 16), lds0 + L_DOWN + it * 1024);
        }
      }
      if (t < 320) *(u32*)(smem + L_ONES + t * 4) = 0x3F803F80u;
      WAITVM(0);
      BAR();
      const int rr = j >> 2, p = j & 3;
      u32x4 agu[2][5];
      if (w < 4) {
#pragma unroll
        for (int mat = 0; mat < 2; ++mat)
#pragma unroll
          for (int kk = 0; kk < 5; ++kk) {
            const u8* p0 = smem + (mat ? L_UP : L_GATE) + (32 * (5 * w + kk) + 8 * q + rr) * 32 + p * 8;
            agu[mat][kk] = cat(trrd(p0), trrd(p0 + 4 * 32));
          }
      }
      u32x2 adn[3][2];
#pragma unroll
      for (int pi = 0; pi < 3; ++pi) {
        int PP = w + 8 * pi; PP = PP < 20 ? PP : w;
#pragma unroll
        for (int tt = 0; tt < 2; ++tt) adn[pi][tt] = trrd(smem + L_DOWN + (4 * q + rr) * 1280 + (32 * PP + 16 * tt + 4 * p) * 2);
      }
      if (w < 3) {
        u32 off[1] = {WS_R6 + (u32)t * 16}; bool act[1] = {t < 160}; u32x4 v[1];
        poll_tagged<1>(rs, off, act, tag, v);
        if (t < 160) *(u32x2*)(smem + L_HB + t * 8) = u32x2{v[0].x, v[0].z};
      }
      BAR();
      if (w < 4) {
        f32x4 ga = zero4, ua = zero4;
        const u8* hbp = smem + ((j == 1) ? L_ONES : L_HB) + (160 * w + 8 * q) * 2;
#pragma unroll
        for (int kk = 0; kk < 5; ++kk) {
          const u32x4 bh = *(const u32x4*)(hbp + kk * 64);
          ga = mfma32(agu[0][kk], bh, ga);
          ua = mfma32(agu[1][kk], bh, ua);
        }
        if (j < 2) {
#pragma unroll
          for (int e = 0; e < 4; ++e) {
            *(float*)(smem + L_GP + ((0 + j) * 16 + 4 * q + e) * 16 + w * 4) = ga[e];
            *(float*)(smem + L_GP + ((2 + j) * 16 + 4 * q + e) * 16 + w * 4) = ua[e];
          }
        }
      } else {
        const float K = bflo(*(const u16*)(smem + L_HB));
        const u32x2 hv = *(const u32x2*)(smem + L_HB + (160 * (w - 4) + 4 * (l < 40 ? l : 0)) * 2);
        float d0 = bflo(hv.x) - K, d1 = bfhi(hv.x) - K, d2 = bflo(hv.y) - K, d3 = bfhi(hv.y) - K;
        float s1 = (d0 + d1) + (d2 + d3), s2 = (d0 * d0 + d1 * d1) + (d2 * d2 + d3 * d3);
        if (l >= 40) { s1 = 0.f; s2 = 0.f; }
        s1 = wave_sum(s1); s2 = wave_sum(s2);
        if (l == 0) {
          *(f32x2*)(smem + L_ST + (w - 4) * 8) = f32x2{s1, s2};
          if (w == 4) *(float*)(smem + L_ST + 32) = K;
        }
      }
      BAR();
      {
        const int mat = (j >> 2) & 1, f = 4 * q + (j & 3);
        const f32x4 hw = *(const f32x4*)(smem + L_GP + ((2 * mat + 0) * 16 + f) * 16);
        const f32x4 cs = *(const f32x4*)(smem + L_GP + ((2 * mat + 1) * 16 + f) * 16);
        const f32x4 s01 = *(const f32x4*)(smem + L_ST), s23 = *(const f32x4*)(smem + L_ST + 16);
        const float K = *(const float*)(smem + L_ST + 32);
        const float m1 = ((s01[0] + s01[2]) + (s23[0] + s23[2])) * (1.0f / 640.0f);
        const float m2 = ((s01[1] + s01[3]) + (s23[1] + s23[3])) * (1.0f / 640.0f);
        const float mean = K + m1;
        const float rstd = 1.0f / sqrtf(fmaxf(m2 - m1 * m1, 0.f) + 1e-5f);
        const float val = rstd * (((hw[0] + hw[1]) + (hw[2] + hw[3])) - mean * ((cs[0] + cs[1]) + (cs[2] + cs[3])));
        const float up = dppf<SHL4>(val);
        const float hh = (val > 0.f ? val : 0.f) * up;
        const u32 pk = cvtpk(hh, dppf<XOR1>(hh));
        const u32x2 bd = {dppu<NBC + 0>(pk), dppu<NBC + 2>(pk)};
#pragma unroll
        for (int pi = 0; pi < 3; ++pi) {
          const int PP = w + 8 * pi;
          if (PP < 20) {
            f32x4 a0 = mfma16(adn[pi][0], bd, zero4), a1 = mfma16(adn[pi][1], bd, zero4);
            f32x4 a = (j & 1) ? a1 : a0;
            if (j < 2) st_dev(rs, WS_R7 + (u32)(PP * 108 + fg) * 128 + (4 * j + q) * 16, granule(a, tag));
          }
        }
      }
    } else {
      const int cg = 4 * (Q - 27) + (idx & 3);
      const u32x2 xv = *(const u32x2*)((const u8*)x + (32 * cg + 4 * (l & 7)) * 2);
      f32x4 hsum = reduce_lines<128>(rs, WS_R5 + (u32)cg * 128 * 128, tag, smem + L_RED5, t, w, l);
      f32x4 hv = {bflo(xv.x) + hsum[0], bfhi(xv.x) + hsum[1], bflo(xv.y) + hsum[2], bfhi(xv.y) + hsum[3]};
      if (w == 0 && l < 8) st_dev(rs, WS_R6 + (u32)cg * 128 + l * 16, granule(hv, tag));
      f32x4 fsum = reduce_lines<108>(rs, WS_R7 + (u32)cg * 108 * 128, tag, smem + L_RED7, t, w, l);
      if (w == 0 && l < 8) {
        hv += fsum;
        *(u32x2*)((u8*)out + (32 * cg + 4 * l) * 2) = u32x2{cvtpk(hv[0], hv[1]), cvtpk(hv[2], hv[3])};
        if (cg == 0 && l == 0) __builtin_amdgcn_raw_buffer_store_b32(tag, rs, 0, 0, 16);
      }
    }
  }
}

extern "C" void kernel_launch(void* const* d_in, const int* in_sizes, int n_in, void* d_out, int out_size, void* d_ws,
                              size_t ws_size, hipStream_t stream) {
  (void)in_sizes; (void)n_in; (void)out_size; (void)ws_size;
  mla_ffn_decode_kernel<<<256, 512, 0, stream>>>((const u16*)d_in[0], (const u16*)d_in[1], (const u16*)d_in[2],
                                                 (const u16*)d_in[3], (const u16*)d_in[4], (const u16*)d_in[5],
                                                 (const u16*)d_in[7], (const u16*)d_in[8], (const u16*)d_in[9],
                                                 (u16*)d_out, (u8*)d_ws);
}
```

```cpp
#include <hip/hip_runtime.h>
#include <hip/hip_bf16.h>

typedef unsigned int u32;
typedef unsigned short u16;
typedef unsigned char u8;
typedef float f32x4 __attribute__((ext_vector_type(4)));
typedef float f32x2 __attribute__((ext_vector_type(2)));
typedef u32 u32x4 __attribute__((ext_vector_type(4)));
typedef u32 u32x2 __attribute__((ext_vector_type(2)));
typedef __bf16 bf16x8 __attribute__((ext_vector_type(8)));
typedef __bf16 bf16x2 __attribute__((ext_vector_type(2)));
typedef short s16x4 __attribute__((ext_vector_type(4)));
typedef __amdgpu_buffer_rsrc_t rsrc_t;

#define DEV __device__ __forceinline__

constexpr u32 WS_R2 = 4096;
constexpr u32 WS_FL = WS_R2 + 65536;
constexpr u32 WS_ML = WS_FL + 16384;
constexpr u32 WS_R6 = WS_ML + 16384;
constexpr u32 WS_R5 = WS_R6 + 4096;
constexpr u32 WS_R7 = WS_R5 + 20 * 128 * 128;
constexpr u32 WS_R3 = WS_R7 + 20 * 108 * 128 + 2048;
constexpr u32 WS_END = WS_R3 + 32 * 32 * 1024;

constexpr u32 L_WUK = 0, L_WUV = 32768, L_WO = 65536, L_QP = 106496, L_MLB = 114688, L_CTX = 114944;
constexpr u32 CKS = 1056;
constexpr u32 L_CKV = 0, L_QL = 128 * CKS, L_WM = L_QL + 8 * CKS, L_LP = L_WM + 256, L_PF = L_LP + 256;
constexpr u32 LDS_BYTES = L_PF + 4096;
constexpr u32 L_GATE = 0, L_UP = 20480, L_DOWN = 40960, L_ONES = 61440, L_HB = 62720, L_GP = 64000, L_ST = 65024;
constexpr u32 L_RED5 = 0, L_RED7 = 1024;

constexpr int PF_LINES = 126;
constexpr int SPIN_MAX = 1 << 14;
constexpr u32 SENT = 0xFFFFFFFFu;

DEV f32x4 mfma32(u32x4 a, u32x4 b, f32x4 c) {
  return __builtin_amdgcn_mfma_f32_16x16x32_bf16(__builtin_bit_cast(bf16x8, a), __builtin_bit_cast(bf16x8, b), c, 0, 0, 0);
}
DEV f32x4 mfma16(u32x2 a, u32x2 b, f32x4 c) {
  return __builtin_amdgcn_mfma_f32_16x16x16bf16_1k(__builtin_bit_cast(s16x4, a), __builtin_bit_cast(s16x4, b), c, 0, 0, 0);
}
DEV u32 cvtpk(float lo, float hi) {
  f32x2 v = {lo, hi};
  return __builtin_bit_cast(u32, __builtin_convertvector(v, bf16x2));
}
DEV float bflo(u32 u) { return __uint_as_float(u << 16); }
DEV float bfhi(u32 u) { return __uint_as_float(u & 0xffff0000u); }
template <int CTRL> DEV u32 dppu(u32 v) { return (u32)__builtin_amdgcn_update_dpp(0, (int)v, CTRL, 0xf, 0xf, true); }
template <int CTRL> DEV float dppf(float v) { return __uint_as_float(dppu<CTRL>(__float_as_uint(v))); }
constexpr int ROR8 = 0x128, ROR4 = 0x124, ROR2 = 0x122, ROR1 = 0x121, XOR1 = 0xB1, SHL4 = 0x104, NBC = 0x150;
DEV float red32(float a, float b) {
  auto s = __builtin_amdgcn_permlane32_swap(__float_as_uint(a), __float_as_uint(b), false, false);
  return __uint_as_float(s[0]) + __uint_as_float(s[1]);
}
DEV float red16(float a, float b) {
  auto s = __builtin_amdgcn_permlane16_swap(__float_as_uint(a), __float_as_uint(b), false, false);
  return __uint_as_float(s[0]) + __uint_as_float(s[1]);
}
DEV float max32(float a) {
  auto s = __builtin_amdgcn_permlane32_swap(__float_as_uint(a), __float_as_uint(a), false, false);
  return fmaxf(__uint_as_float(s[0]), __uint_as_float(s[1]));
}
DEV float max16(float a) {
  auto s = __builtin_amdgcn_permlane16_swap(__float_as_uint(a), __float_as_uint(a), false, false);
  return fmaxf(__uint_as_float(s[0]), __uint_as_float(s[1]));
}
DEV float wave_sum(float v) {
  v += dppf<ROR8>(v); v += dppf<ROR4>(v); v += dppf<ROR2>(v); v += dppf<ROR1>(v);
  v = red16(v, v);
  return red32(v, v);
}
DEV u32x2 trrd(const u8* p) {
  typedef short v4i16_t __attribute__((ext_vector_type(4)));
  return __builtin_bit_cast(u32x2, __builtin_amdgcn_ds_read_tr16_b64_v4i16((__attribute__((address_space(3))) v4i16_t*)p));
}
DEV u32x4 cat(u32x2 a, u32x2 b) { return u32x4{a.x, a.y, b.x, b.y}; }
DEV void dma16(const void* sbase, u32 voff, u32 lds) {
  u32 keep;
  asm volatile("s_mov_b32 %0, m0\n\ts_mov_b32 m0, %3\n\ts_nop 0\n\tglobal_load_lds_dwordx4 %1, %2\n\ts_mov_b32 m0, %0"
               : "=&s"(keep) : "v"(voff), "s"(sbase), "s"(lds) : "memory");
}
#define BAR() asm volatile("s_waitcnt lgkmcnt(0)\n\ts_barrier" ::: "memory")
#define WAITVM(n) asm volatile("s_waitcnt vmcnt(" #n ")" ::: "memory")
DEV u32x4 ld_dev(rsrc_t rs, u32 off) { return __builtin_amdgcn_raw_buffer_load_b128(rs, off, 0, 16); }
DEV void st_dev(rsrc_t rs, u32 off, u32x4 v) { __builtin_amdgcn_raw_buffer_store_b128(v, rs, off, 0, 16); }
DEV bool wave_any(bool p) { return __builtin_amdgcn_ballot_w64(p) != 0; }

template <int N> DEV void poll_tagged(rsrc_t rs, const u32 (&off)[N], const bool (&act)[N], u32 tag, u32x4 (&v)[N]) {
  for (int spin = 0; spin < SPIN_MAX; ++spin) {
    bool bad = false;
    asm volatile("" ::: "memory");
#pragma unroll
    for (int i = 0; i < N; ++i) {
      v[i] = u32x4{0, tag, 0, tag};
      if (act[i]) v[i] = ld_dev(rs, off[i]);
    }
#pragma unroll
    for (int i = 0; i < N; ++i) bad |= (v[i].y != tag) | (v[i].w != tag);
    if (!wave_any(bad)) break;
  }
}
DEV u32x4 granule(f32x4 a, u32 tag) { return u32x4{cvtpk(a[0], a[1]), tag, cvtpk(a[2], a[3]), tag}; }

template <int NP> DEV f32x4 reduce_lines(rsrc_t rs, u32 base, u32 tag, u8* red, int t, int w, int l) {
  u32 off[2]; bool act[2]; u32x4 v[2];
#pragma unroll
  for (int i = 0; i < 2; ++i) {
    int p = (t >> 3) + 64 * i;
    act[i] = p < NP;
    off[i] = base + p * 128 + (t & 7) * 16;
  }
  poll_tagged<2>(rs, off, act, tag, v);
  float a0 = bflo(v[0].x) + bflo(v[1].x), a1 = bfhi(v[0].x) + bfhi(v[1].x);
  float a2 = bflo(v[0].z) + bflo(v[1].z), a3 = bfhi(v[0].z) + bfhi(v[1].z);
  float r0 = red32(a0, a2), r1 = red32(a1, a3);
  float r = red16(r0, r1);
  r += dppf<ROR8>(r);
  if ((l & 8) == 0) *(float*)(red + w * 128 + (4 * (l & 7) + (l >> 4)) * 4) = r;
  BAR();
  f32x4 s = {0.f, 0.f, 0.f, 0.f};
  if (w == 0) {
#pragma unroll
    for (int i = 0; i < 8; ++i) s += *(const f32x4*)(red + i * 128 + (l & 7) * 16);
  }
  return s;
}

__global__ void __launch_bounds__(512, 2)
mla_ffn_decode_kernel(const u16* __restrict__ x, const u16* __restrict__ ckv, const u16* __restrict__ wq,
                      const u16* __restrict__ wuk, const u16* __restrict__ wuv, const u16* __restrict__ wo,
                      const u16* __restrict__ wg, const u16* __restrict__ wu, const u16* __restrict__ wd,
                      u16* __restrict__ out, u8* __restrict__ ws) {
  __shared__ __attribute__((aligned(16))) u8 smem[LDS_BYTES + 256];
  const u32 lds0 = (u32)(size_t)(__attribute__((address_space(3))) u8*)smem;
  const int b = blockIdx.x, xc = b & 7, r = b >> 3, idx = r & 15;
  const int t = threadIdx.x, l = t & 63, q = l >> 4, j = l & 15;
  const int w = __builtin_amdgcn_readfirstlane(t >> 6);
  const rsrc_t rs = __builtin_amdgcn_make_buffer_rsrc((void*)ws, 0, WS_END, 0x00020000);
  const u32 epoch_v = __builtin_amdgcn_raw_buffer_load_b32(rs, 0, 0, 16);
  const f32x4 zero4 = {0.f, 0.f, 0.f, 0.f};
  if (w == 7) {
    u32 pf_lo, pf_hi;
    asm volatile("s_getpc_b64 s[20:21]\n\ts_mov_b32 %0, s20\n\ts_mov_b32 %1, s21" : "=s"(pf_lo), "=s"(pf_hi) : : "s20", "s21");
    const u8* pc = (const u8*)(((unsigned long long)pf_hi << 32) | (pf_lo & ~127u));
    u32 keep;
    if (l < 4 && r * 4 + l < PF_LINES)
      asm volatile("s_mov_b32 %0, m0\n\ts_mov_b32 m0, %3\n\ts_nop 0\n\tglobal_load_lds_dword %1, %2\n\ts_mov_b32 m0, %0"
               : "=&s"(keep) : "v"((u32)((r * 4 + l) * 128)), "s"(pc), "s"(lds0 + LDS_BYTES) : "memory");
  }

  if (r < 16) {
    const int h = 4 * xc + (idx >> 2), cq = idx & 3;
    if (w < 4) {
#pragma unroll
      for (int n = 0; n < 8; ++n) {
        int row = 32 * w + 4 * n + q;
        dma16(wuk, (u32)(((128 * cq + row) * 32 + h) * 256 + ((j ^ (row & 15)) * 16)), lds0 + L_WUK + (32 * w + 4 * n) * 256);
      }
    }
    const u32x4 xa = *(const u32x4*)((const u8*)x + l * 16);
    const u32 xb = *(const u32*)((const u8*)x + 1024 + l * 4);
    u32x2 xr[5];
#pragma unroll
    for (int k = 0; k < 5; ++k) xr[k] = *(const u32x2*)((const u8*)x + (k * 128 + (4 * w + q) * 4) * 2);
    u32x4 wv[20];
    {
      const u8* base = (const u8*)wq + h * 256;
      const u32 vo = (u32)((4 * w + q) * 4 * 8192 + j * 16);
#pragma unroll
      for (int i = 0; i < 20; ++i) wv[i] = *(const u32x4*)(base + ((i >> 2) * 128 + (i & 3)) * 8192 + vo);
    }
    __builtin_amdgcn_sched_barrier(0);
    WAITVM(27);
    u32x4 auk[2][4];
    if (w < 4) {
#pragma unroll
      for (int tt = 0; tt < 2; ++tt)
#pragma unroll
        for (int ks = 0; ks < 4; ++ks)
          auk[tt][ks] = *(const u32x4*)(smem + L_WUK + (32 * w + 16 * tt + j) * 256 + (((4 * ks + q) ^ j) * 16));
    }
    float mean, rstd;
    {
      float v[10] = {bflo(xa.x), bfhi(xa.x), bflo(xa.y), bfhi(xa.y), bflo(xa.z), bfhi(xa.z), bflo(xa.w), bfhi(xa.w), bflo(xb), bfhi(xb)};
      float s = 0.f;
#pragma unroll
      for (int i = 0; i < 10; ++i) s += v[i];
      mean = wave_sum(s) * (1.0f / 640.0f);
      float s2 = 0.f;
#pragma unroll
      for (int i = 0; i < 10; ++i) { float d = v[i] - mean; s2 += d * d; }
      rstd = 1.0f / sqrtf(wave_sum(s2) * (1.0f / 640.0f) + 1e-5f);
    }
    {
      u32 sel[4][2];
#pragma unroll
      for (int k = 0; k < 4; ++k) {
        bool own = ((j & 7) >> 1) == k;
        sel[k][0] = own ? ((j & 1) ? 0x01000c0cu : 0x0c0c0100u) : 0x0c0c0c0cu;
        sel[k][1] = own ? ((j & 1) ? 0x03020c0cu : 0x0c0c0302u) : 0x0c0c0c0cu;
      }
      f32x4 acc0 = zero4, acc1 = zero4;
#pragma unroll
      for (int i = 0; i < 20; ++i) {
        u32 xw = (i & 2) ? xr[i >> 2].y : xr[i >> 2].x;
        xw = (j < 8) ? xw : 0x3F803F80u;
        u32x4 bf;
#pragma unroll
        for (int k = 0; k < 4; ++k) bf[k] = __builtin_amdgcn_perm(xw, xw, sel[k][i & 1]);
        if (i & 1) acc1 = mfma32(wv[i], bf, acc1); else acc0 = mfma32(wv[i], bf, acc0);
      }
      acc0 += acc1;
#pragma unroll
      for (int e = 0; e < 4; ++e) {
        int P = 16 * q + 4 * e + ((j & 7) >> 1);
        int pos = P ^ ((q & 1) << 3);
        *(float*)(smem + L_QP + w * 1024 + pos * 16 + (j >> 3) * 8 + (j & 1) * 4) = acc0[e];
      }
    }
    BAR();
    const u32 tag = __builtin_amdgcn_readfirstlane(epoch_v) + 1u;
    if (w < 4) {
      const int P = 16 * (j >> 2) + 4 * q + (j & 3);
      const int pos = P ^ (((j >> 2) & 1) << 3);
      f32x4 s = zero4;
#pragma unroll
      for (int i = 0; i < 8; ++i) s += *(const f32x4*)(smem + L_QP + i * 1024 + pos * 16);
      const u32 pk = cvtpk(rstd * (s[0] - mean * s[2]), rstd * (s[1] - mean * s[3]));
      u32x4 bq[4];
      bq[0] = u32x4{dppu<NBC + 0>(pk), dppu<NBC + 1>(pk), dppu<NBC + 2>(pk), dppu<NBC + 3>(pk)};
      bq[1] = u32x4{dppu<NBC + 4>(pk), dppu<NBC + 5>(pk), dppu<NBC + 6>(pk), dppu<NBC + 7>(pk)};
      bq[2] = u32x4{dppu<NBC + 8>(pk), dppu<NBC + 9>(pk), dppu<NBC + 10>(pk), dppu<NBC + 11>(pk)};
      bq[3] = u32x4{dppu<NBC + 12>(pk), dppu<NBC + 13>(pk), dppu<NBC + 14>(pk), dppu<NBC + 15>(pk)};
      f32x4 a0 = zero4, a1 = zero4;
#pragma unroll
      for (int ks = 0; ks < 4; ++ks) { a0 = mfma32(auk[0][ks], bq[ks], a0); a1 = mfma32(auk[1][ks], bq[ks], a1); }
      const float sc = 0.08838834764831845f * 1.4426950408889634f;
      f32x4 a = (j & 1) ? a1 : a0;
      a *= sc;
      if (j < 2) st_dev(rs, WS_R2 + h * 2048 + (4 * cq + w) * 128 + (4 * j + q) * 16, granule(a, tag));
    }
    BAR();
    const int jq = cq, g = h >> 3;
#pragma unroll
    for (int n = 0; n < 4; ++n) {
      int row = 64 * w + 16 * n + (l >> 2);
      dma16(wuv, (u32)((row * 32 + h) * 256 + jq * 64 + (l & 3) * 16), lds0 + L_WUV + (64 * w + 16 * n) * 64);
    }
#pragma unroll
    for (int n = 0; n < 5; ++n)
      dma16(wo, (u32)((h * 128 + 32 * jq) * 1280 + (5 * w + n) * 1024 + l * 16), lds0 + L_WO + (5 * w + n) * 1024);
    if (w == 0) {
      u32 off[1] = {WS_FL + (u32)(g * 32 + (l & 31)) * 128}; bool act[1] = {true}; u32x4 v[1];
      poll_tagged<1>(rs, off, act, tag, v);
    }
    WAITVM(0);
    BAR();
    u32x4 buv[2][2];
    {
      const int rr = j >> 2, p = j & 3;
      const int c0 = 64 * w + 16 * (q >> 1) + 2 * (q & 1) + 32 * (rr & 1) + 4 * (rr >> 1);
#pragma unroll
      for (int dt = 0; dt < 2; ++dt)
#pragma unroll
        for (int a = 0; a < 2; ++a) {
          const u8* p0 = smem + L_WUV + (c0 + a) * 64 + dt * 32 + p * 8;
          buv[dt][a] = cat(trrd(p0), trrd(p0 + 8 * 64));
        }
    }
    u32x4 awo[3][2];
    {
      const int rr = j >> 2, p = j & 3;
#pragma unroll
      for (int pi = 0; pi < 3; ++pi) {
        int PP = w + 8 * pi; PP = PP < 20 ? PP : w;
#pragma unroll
        for (int tt = 0; tt < 2; ++tt) {
          const u8* p0 = smem + L_WO + (8 * q + rr) * 1280 + (32 * PP + 16 * tt + 4 * p) * 2;
          awo[pi][tt] = cat(trrd(p0), trrd(p0 + 4 * 1280));
        }
      }
    }
    {
      u32 off[1] = {WS_ML + (u32)(g * 32 + 4 * w + (l & 3)) * 128 + (h & 7) * 16}; bool act[1] = {true}; u32x4 v[1];
      poll_tagged<1>(rs, off, act, tag, v);
      int s = 4 * w + l;
      if (l < 4) *(u32x2*)(smem + L_MLB + (s & 7) * 32 + (s >> 3) * 8) = u32x2{v[0].x, v[0].z};
    }
    BAR();
    float e[4], inv;
    {
      const f32x4 m01 = *(const f32x4*)(smem + L_MLB + (l >> 3) * 32), m23 = *(const f32x4*)(smem + L_MLB + (l >> 3) * 32 + 16);
      float mx = fmaxf(fmaxf(m01[0], m01[2]), fmaxf(m23[0], m23[2]));
      mx = fmaxf(mx, dppf<ROR8>(mx)); mx = max16(mx); mx = max32(mx);
      e[0] = __builtin_amdgcn_exp2f(m01[0] - mx); e[1] = __builtin_amdgcn_exp2f(m01[2] - mx);
      e[2] = __builtin_amdgcn_exp2f(m23[0] - mx); e[3] = __builtin_amdgcn_exp2f(m23[2] - mx);
      float L = e[0] * m01[1] + e[1] * m01[3] + e[2] * m23[1] + e[3] * m23[3];
      L += dppf<ROR8>(L); L = red16(L, L); L = red32(L, L);
      inv = 1.0f / L;
    }
    u32x4 v3[4];
    {
      const u32 o = WS_R3 + (u32)(h * 32 + (l >> 3)) * 1024 + w * 128 + (l & 7) * 16;
      for (int spin = 0; spin < SPIN_MAX; ++spin) {
        bool bad = false;
        asm volatile("" ::: "memory");
#pragma unroll
        for (int i = 0; i < 4; ++i) v3[i] = ld_dev(rs, o + i * 8192);
#pragma unroll
        for (int i = 0; i < 4; ++i) bad |= (v3[i].x == SENT) | (v3[i].y == SENT) | (v3[i].z == SENT) | (v3[i].w == SENT);
        if (!wave_any(bad)) break;
      }
    }
    {
      float acc[8];
#pragma unroll
      for (int k = 0; k < 4; ++k) { acc[2 * k] = e[0] * bflo(v3[0][k]); acc[2 * k + 1] = e[0] * bfhi(v3[0][k]); }
#pragma unroll
      for (int i = 1; i < 4; ++i)
#pragma unroll
        for (int k = 0; k < 4; ++k) { acc[2 * k] += e[i] * bflo(v3[i][k]); acc[2 * k + 1] += e[i] * bfhi(v3[i][k]); }
      float r4[4], r2[2];
#pragma unroll
      for (int k = 0; k < 4; ++k) r4[k] = red32(acc[k], acc[k + 4]);
#pragma unroll
      for (int k = 0; k < 2; ++k) r2[k] = red16(r4[k], r4[k + 2]);
      const bool ah = (l & 8) != 0;
      float keep = ah ? r2[1] : r2[0], give = ah ? r2[0] : r2[1];
      float val = (keep + dppf<ROR8>(give)) * inv;
      const u32 pk = cvtpk(val, val);
      const u32 hm = (l & 1) ? 0xffff0000u : 0x0000ffffu;
      f32x4 d0 = zero4, d1 = zero4;
#pragma unroll
      for (int a = 0; a < 2; ++a) {
        u32x4 af;
#pragma unroll
        for (int k = 0; k < 4; ++k) af[k] = pk & ((((l & 7) >> 1) == k && (int)ah == a) ? hm : 0u);
        d0 = mfma32(af, buv[0][a], d0);
        d1 = mfma32(af, buv[1][a], d1);
      }
      float s0 = (d0[0] + d0[1]) + (d0[2] + d0[3]), s1 = (d1[0] + d1[1]) + (d1[2] + d1[3]);
      float s = red32(s0, s1);
      s = red16(s, s);
      if ((q & 1) == 0) *(float*)(smem + L_CTX + (16 * (l >> 5) + j) * 32 + w * 4) = s;
    }
    BAR();
    {
      const int d = 8 * q + (j & 7);
      const f32x4 c0 = *(const f32x4*)(smem + L_CTX + d * 32), c1 = *(const f32x4*)(smem + L_CTX + d * 32 + 16);
      const f32x4 cs = c0 + c1;
      const float v = (cs[0] + cs[1]) + (cs[2] + cs[3]);
      const u32 pk = cvtpk(v, dppf<XOR1>(v));
      const u32x4 bc = {dppu<NBC + 0>(pk), dppu<NBC + 2>(pk), dppu<NBC + 4>(pk), dppu<NBC + 6>(pk)};
      const int p = h * 4 + jq;
#pragma unroll
      for (int pi = 0; pi < 3; ++pi) {
        const int PP = w + 8 * pi;
        if (PP < 20) {
          f32x4 a0 = mfma32(awo[pi][0], bc, zero4), a1 = mfma32(awo[pi][1], bc, zero4);
          f32x4 a = (j & 1) ? a1 : a0;
          if (j < 2) st_dev(rs, WS_R5 + (u32)(PP * 128 + p) * 128 + (4 * j + q) * 16, granule(a, tag));
        }
      }
    }
  } else {
    const int g = idx & 3, s = 4 * xc + (idx >> 2);
    const u32 off3 = WS_R3 + (u32)((8 * g + (j & 7)) * 32 + s) * 1024 + w * 128 + 32 * q + 16 * (j >> 3);
    st_dev(rs, off3, u32x4{SENT, SENT, SENT, SENT});
    {
      const u8* base = (const u8*)ckv + (size_t)(128 * s + 16 * w) * 1024;
#pragma unroll
      for (int n = 0; n < 16; ++n) dma16(base + n * 1024, (u32)(l * 16), lds0 + L_CKV + (16 * w + n) * CKS);
    }
    const int Q = xc + 8 * (idx >> 2);
    WAITVM(0);
    BAR();
    const u32 tag = __builtin_amdgcn_readfirstlane(epoch_v) + 1u;
    if (w == 0 && l < 8) st_dev(rs, WS_FL + (u32)(g * 32 + s) * 128 + l * 16, u32x4{tag, tag, tag, tag});
    u32x4 aqk[16];
#pragma unroll
    for (int ks = 0; ks < 16; ++ks) aqk[ks] = *(const u32x4*)(smem + L_CKV + (16 * w + j) * CKS + ks * 64 + 16 * q);
    u32x4 apv[4][4];
    {
      const int rr = j >> 2, p = j & 3;
      const u8* p0 = smem + L_CKV + (4 * q + rr) * CKS + (64 * w + 4 * p) * 2;
#pragma unroll
      for (int kt = 0; kt < 4; ++kt)
#pragma unroll
        for (int tt = 0; tt < 4; ++tt)
          apv[kt][tt] = cat(trrd(p0 + (32 * kt) * CKS + tt * 32), trrd(p0 + (32 * kt + 16) * CKS + tt * 32));
    }
    {
      u32 off[2]; bool act[2] = {true, true}; u32x4 v[2];
#pragma unroll
      for (int i = 0; i < 2; ++i) off[i] = WS_R2 + (u32)(8 * g) * 2048 + (t + 512 * i) * 16;
      poll_tagged<2>(rs, off, act, tag, v);
#pragma unroll
      for (int i = 0; i < 2; ++i) {
        int n = t + 512 * i;
        *(u32x2*)(smem + L_QL + (n >> 7) * CKS + (n & 127) * 8) = u32x2{v[i].x, v[i].z};
      }
    }
    BAR();
    f32x4 sc;
    {
      f32x4 lo = zero4, hi = zero4;
      const u8* qb = smem + L_QL + (j & 7) * CKS + (j >> 3) * 512 + 16 * q;
#pragma unroll
      for (int u = 0; u < 8; ++u) {
        const u32x4 bq = *(const u32x4*)(qb + u * 64);
        lo = mfma32(aqk[u], bq, lo);
        hi = mfma32(aqk[u + 8], bq, hi);
      }
#pragma unroll
      for (int e = 0; e < 4; ++e) { float v = (j < 8) ? lo[e] : hi[e]; sc[e] = v + dppf<ROR8>(v); }
    }
    {
      float m = fmaxf(fmaxf(sc[0], sc[1]), fmaxf(sc[2], sc[3]));
      m = max16(m); m = max32(m);
      if (l < 8) *(float*)(smem + L_WM + l * 32 + w * 4) = m;
    }
    BAR();
    float mx;
    {
      const f32x4 a = *(const f32x4*)(smem + L_WM + (j & 7) * 32), c = *(const f32x4*)(smem + L_WM + (j & 7) * 32 + 16);
      mx = fmaxf(fmaxf(fmaxf(a[0], a[1]), fmaxf(a[2], a[3])), fmaxf(fmaxf(c[0], c[1]), fmaxf(c[2], c[3])));
      float p0 = __builtin_amdgcn_exp2f(sc[0] - mx), p1 = __builtin_amdgcn_exp2f(sc[1] - mx);
      float p2 = __builtin_amdgcn_exp2f(sc[2] - mx), p3 = __builtin_amdgcn_exp2f(sc[3] - mx);
      *(u32x2*)(smem + L_PF + (w >> 1) * 1024 + l * 16 + (w & 1) * 8) = u32x2{cvtpk(p0, p1), cvtpk(p2, p3)};
      float ls = (p0 + p1) + (p2 + p3);
      ls = red16(ls, ls); ls = red32(ls, ls);
      if (l < 8) *(float*)(smem + L_LP + l * 32 + w * 4) = ls;
    }
    BAR();
    {
      u32x4 pf[4];
#pragma unroll
      for (int kt = 0; kt < 4; ++kt) pf[kt] = *(const u32x4*)(smem + L_PF + kt * 1024 + l * 16);
      if (w == 0) {
        const f32x4 a = *(const f32x4*)(smem + L_LP + (j & 7) * 32), c = *(const f32x4*)(smem + L_LP + (j & 7) * 32 + 16);
        const f32x4 u = a + c;
        const float lt = (u[0] + u[1]) + (u[2] + u[3]);
        if (l < 8) st_dev(rs, WS_ML + (u32)(g * 32 + s) * 128 + l * 16, u32x4{__float_as_uint(mx), tag, __float_as_uint(lt), tag});
      }
      f32x4 o[4] = {zero4, zero4, zero4, zero4};
#pragma unroll
      for (int kt = 0; kt < 4; ++kt)
#pragma unroll
        for (int tt = 0; tt < 4; ++tt) o[tt] = mfma32(apv[kt][tt], pf[kt], o[tt]);
      const bool hb = j >= 8;
      const f32x4 oa = hb ? o[2] : o[0], ob = hb ? o[3] : o[1];
      u32x4 pv = {cvtpk(oa[0], oa[1]), cvtpk(oa[2], oa[3]), cvtpk(ob[0], ob[1]), cvtpk(ob[2], ob[3])};
#pragma unroll
      for (int k = 0; k < 4; ++k) pv[k] = pv[k] < 0xFFFFFFFEu ? pv[k] : 0xFFFFFFFEu;
      st_dev(rs, off3, pv);
    }
    if (Q < 27) {
      const int fg = 4 * Q + (idx & 3);
#pragma unroll
      for (int n = 0; n < 3; ++n) {
        const int it = w + 8 * n;
        if (it < 20) {
          const u32 vo = (u32)((32 * it + (l >> 1)) * 3456 + fg * 32 + (l & 1) * 16);
          dma16(wg, vo, lds0 + L_GATE + it * 1024);
          dma16(wu, vo, lds0 + L_UP + it * 1024);
          dma16(wd, (u32)(fg * 16 * 1280 + it * 1024 + l * 16), lds0 + L_DOWN + it * 1024);
        }
      }
      if (t < 320) *(u32*)(smem + L_ONES + t * 4) = 0x3F803F80u;
      WAITVM(0);
      BAR();
      const int rr = j >> 2, p = j & 3;
      u32x4 agu[2][5];
      if (w < 4) {
#pragma unroll
        for (int mat = 0; mat < 2; ++mat)
#pragma unroll
          for (int kk = 0; kk < 5; ++kk) {
            const u8* p0 = smem + (mat ? L_UP : L_GATE) + (32 * (5 * w + kk) + 8 * q + rr) * 32 + p * 8;
            agu[mat][kk] = cat(trrd(p0), trrd(p0 + 4 * 32));
          }
      }
      u32x2 adn[3][2];
#pragma unroll
      for (int pi = 0; pi < 3; ++pi) {
        int PP = w + 8 * pi; PP = PP < 20 ? PP : w;
#pragma unroll
        for (int tt = 0; tt < 2; ++tt) adn[pi][tt] = trrd(smem + L_DOWN + (4 * q + rr) * 1280 + (32 * PP + 16 * tt + 4 * p) * 2);
      }
      if (w < 3) {
        u32 off[1] = {WS_R6 + (u32)t * 16}; bool act[1] = {t < 160}; u32x4 v[1];
        poll_tagged<1>(rs, off, act, tag, v);
        if (t < 160) *(u32x2*)(smem + L_HB + t * 8) = u32x2{v[0].x, v[0].z};
      }
      BAR();
      if (w < 4) {
        f32x4 ga = zero4, ua = zero4;
        const u8* hbp = smem + ((j == 1) ? L_ONES : L_HB) + (160 * w + 8 * q) * 2;
#pragma unroll
        for (int kk = 0; kk < 5; ++kk) {
          const u32x4 bh = *(const u32x4*)(hbp + kk * 64);
          ga = mfma32(agu[0][kk], bh, ga);
          ua = mfma32(agu[1][kk], bh, ua);
        }
        if (j < 2) {
#pragma unroll
          for (int e = 0; e < 4; ++e) {
            *(float*)(smem + L_GP + ((0 + j) * 16 + 4 * q + e) * 16 + w * 4) = ga[e];
            *(float*)(smem + L_GP + ((2 + j) * 16 + 4 * q + e) * 16 + w * 4) = ua[e];
          }
        }
      } else {
        const float K = bflo(*(const u16*)(smem + L_HB));
        const u32x2 hv = *(const u32x2*)(smem + L_HB + (160 * (w - 4) + 4 * (l < 40 ? l : 0)) * 2);
        float d0 = bflo(hv.x) - K, d1 = bfhi(hv.x) - K, d2 = bflo(hv.y) - K, d3 = bfhi(hv.y) - K;
        float s1 = (d0 + d1) + (d2 + d3), s2 = (d0 * d0 + d1 * d1) + (d2 * d2 + d3 * d3);
        if (l >= 40) { s1 = 0.f; s2 = 0.f; }
        s1 = wave_sum(s1); s2 = wave_sum(s2);
        if (l == 0) {
          *(f32x2*)(smem + L_ST + (w - 4) * 8) = f32x2{s1, s2};
          if (w == 4) *(float*)(smem + L_ST + 32) = K;
        }
      }
      BAR();
      {
        const int mat = (j >> 2) & 1, f = 4 * q + (j & 3);
        const f32x4 hw = *(const f32x4*)(smem + L_GP + ((2 * mat + 0) * 16 + f) * 16);
        const f32x4 cs = *(const f32x4*)(smem + L_GP + ((2 * mat + 1) * 16 + f) * 16);
        const f32x4 s01 = *(const f32x4*)(smem + L_ST), s23 = *(const f32x4*)(smem + L_ST + 16);
        const float K = *(const float*)(smem + L_ST + 32);
        const float m1 = ((s01[0] + s01[2]) + (s23[0] + s23[2])) * (1.0f / 640.0f);
        const float m2 = ((s01[1] + s01[3]) + (s23[1] + s23[3])) * (1.0f / 640.0f);
        const float mean = K + m1;
        const float rstd = 1.0f / sqrtf(fmaxf(m2 - m1 * m1, 0.f) + 1e-5f);
        const float val = rstd * (((hw[0] + hw[1]) + (hw[2] + hw[3])) - mean * ((cs[0] + cs[1]) + (cs[2] + cs[3])));
        const float up = dppf<SHL4>(val);
        const float hh = (val > 0.f ? val : 0.f) * up;
        const u32 pk = cvtpk(hh, dppf<XOR1>(hh));
        const u32x2 bd = {dppu<NBC + 0>(pk), dppu<NBC + 2>(pk)};
#pragma unroll
        for (int pi = 0; pi < 3; ++pi) {
          const int PP = w + 8 * pi;
          if (PP < 20) {
            f32x4 a0 = mfma16(adn[pi][0], bd, zero4), a1 = mfma16(adn[pi][1], bd, zero4);
            f32x4 a = (j & 1) ? a1 : a0;
            if (j < 2) st_dev(rs, WS_R7 + (u32)(PP * 108 + fg) * 128 + (4 * j + q) * 16, granule(a, tag));
          }
        }
      }
    } else {
      const int cg = 4 * (Q - 27) + (idx & 3);
      const rsrc_t ro = __builtin_amdgcn_make_buffer_rsrc((void*)out, 0, 1280, 0x00020000);
      const u32x2 xv = *(const u32x2*)((const u8*)x + (32 * cg + 4 * (l & 7)) * 2);
      f32x4 hsum = reduce_lines<128>(rs, WS_R5 + (u32)cg * 128 * 128, tag, smem + L_RED5, t, w, l);
      f32x4 hv = {bflo(xv.x) + hsum[0], bfhi(xv.x) + hsum[1], bflo(xv.y) + hsum[2], bfhi(xv.y) + hsum[3]};
      if (w == 0 && l < 8) st_dev(rs, WS_R6 + (u32)cg * 128 + l * 16, granule(hv, tag));
      f32x4 fsum = reduce_lines<108>(rs, WS_R7 + (u32)cg * 108 * 128, tag, smem + L_RED7, t, w, l);
      if (w == 0 && l < 8) {
        hv += fsum;
        __builtin_amdgcn_raw_buffer_store_b64(u32x2{cvtpk(hv[0], hv[1]), cvtpk(hv[2], hv[3])}, ro, (32 * cg + 4 * l) * 2, 0, 16);
        if (cg == 0 && l == 0) __builtin_amdgcn_raw_buffer_store_b32(tag, rs, 0, 0, 16);
      }
    }
  }
}

extern "C" void kernel_launch(void* const* d_in, const int* in_sizes, int n_in, void* d_out, int out_size, void* d_ws,
                              size_t ws_size, hipStream_t stream) {
  (void)in_sizes; (void)n_in; (void)out_size; (void)ws_size;
  mla_ffn_decode_kernel<<<256, 512, 0, stream>>>((const u16*)d_in[0], (const u16*)d_in[1], (const u16*)d_in[2],
                                                 (const u16*)d_in[3], (const u16*)d_in[4], (const u16*)d_in[5],
                                                 (const u16*)d_in[7], (const u16*)d_in[8], (const u16*)d_in[9],
                                                 (u16*)d_out, (u8*)d_ws);
}
```

```cpp
#include <hip/hip_runtime.h>
#include <hip/hip_bf16.h>

typedef unsigned int u32;
typedef unsigned short u16;
typedef unsigned char u8;
typedef float f32x4 __attribute__((ext_vector_type(4)));
typedef float f32x2 __attribute__((ext_vector_type(2)));
typedef u32 u32x4 __attribute__((ext_vector_type(4)));
typedef u32 u32x2 __attribute__((ext_vector_type(2)));
typedef __bf16 bf16x8 __attribute__((ext_vector_type(8)));
typedef __bf16 bf16x2 __attribute__((ext_vector_type(2)));
typedef short s16x4 __attribute__((ext_vector_type(4)));
typedef __amdgpu_buffer_rsrc_t rsrc_t;

#define DEV __device__ __forceinline__

constexpr u32 LS = 384;
constexpr u32 WS_PF = 4096;
constexpr u32 WS_R2 = WS_PF + 32 * LS;
constexpr u32 WS_FL = WS_R2 + 512 * LS;
constexpr u32 WS_ML = WS_FL + 128 * LS;
constexpr u32 WS_R6 = WS_ML + 128 * LS;
constexpr u32 WS_R5 = WS_R6 + 128 * LS;
constexpr u32 WS_R7 = WS_R5 + 20 * 128 * LS;
constexpr u32 WS_R3 = WS_R7 + 20 * 108 * LS + 16 * LS;
constexpr u32 WS_END = WS_R3 + 32 * 32 * 8 * LS;

constexpr u32 L_WUK = 0, L_WUV = 32768, L_WO = 65536, L_QP = 106496, L_MLB = 114688, L_CTX = 114944;
constexpr u32 CKS = 1056;
constexpr u32 L_CKV = 0, L_QL = 128 * CKS, L_WM = L_QL + 8 * CKS, L_LP = L_WM + 256, L_PF = L_LP + 256;
constexpr u32 LDS_BYTES = L_PF + 4096;
constexpr u32 L_GATE = 0, L_UP = 20480, L_DOWN = 40960, L_ONES = 61440, L_HB = 62720, L_GP = 64000, L_ST = 65024;
constexpr u32 L_RED5 = 0, L_RED7 = 1024;

constexpr int PF_LINES = 135;
constexpr int SPIN_MAX = 1 << 14;
constexpr u32 SENT = 0xFFFFFFFFu;
constexpr u32 OOB = 0xFFFFFF00u;

DEV f32x4 mfma32(u32x4 a, u32x4 b, f32x4 c) {
  return __builtin_amdgcn_mfma_f32_16x16x32_bf16(__builtin_bit_cast(bf16x8, a), __builtin_bit_cast(bf16x8, b), c, 0, 0, 0);
}
DEV f32x4 mfma16(u32x2 a, u32x2 b, f32x4 c) {
  return __builtin_amdgcn_mfma_f32_16x16x16bf16_1k(__builtin_bit_cast(s16x4, a), __builtin_bit_cast(s16x4, b), c, 0, 0, 0);
}
DEV u32 cvtpk(float lo, float hi) {
  f32x2 v = {lo, hi};
  return __builtin_bit_cast(u32, __builtin_convertvector(v, bf16x2));
}
DEV float bflo(u32 u) { return __uint_as_float(u << 16); }
DEV float bfhi(u32 u) { return __uint_as_float(u & 0xffff0000u); }
template <int CTRL> DEV u32 dppu(u32 v) { return (u32)__builtin_amdgcn_update_dpp(0, (int)v, CTRL, 0xf, 0xf, true); }
template <int CTRL> DEV float dppf(float v) { return __uint_as_float(dppu<CTRL>(__float_as_uint(v))); }
constexpr int HMIR = 0x141, XOR2 = 0x4E;
constexpr int ROR8 = 0x128, ROR4 = 0x124, ROR2 = 0x122, ROR1 = 0x121, XOR1 = 0xB1, SHL4 = 0x104, NBC = 0x150;
DEV float red32(float a, float b) {
  auto s = __builtin_amdgcn_permlane32_swap(__float_as_uint(a), __float_as_uint(b), false, false);
  return __uint_as_float(s[0]) + __uint_as_float(s[1]);
}
DEV float red16(float a, float b) {
  auto s = __builtin_amdgcn_permlane16_swap(__float_as_uint(a), __float_as_uint(b), false, false);
  return __uint_as_float(s[0]) + __uint_as_float(s[1]);
}
DEV float max32(float a) {
  auto s = __builtin_amdgcn_permlane32_swap(__float_as_uint(a), __float_as_uint(a), false, false);
  return fmaxf(__uint_as_float(s[0]), __uint_as_float(s[1]));
}
DEV float max16(float a) {
  auto s = __builtin_amdgcn_permlane16_swap(__float_as_uint(a), __float_as_uint(a), false, false);
  return fmaxf(__uint_as_float(s[0]), __uint_as_float(s[1]));
}
DEV float wave_sum(float v) {
  v += dppf<ROR8>(v); v += dppf<ROR4>(v); v += dppf<ROR2>(v); v += dppf<ROR1>(v);
  v = red16(v, v);
  return red32(v, v);
}
DEV u32x2 trrd(const u8* p) {
  typedef short v4i16_t __attribute__((ext_vector_type(4)));
  return __builtin_bit_cast(u32x2, __builtin_amdgcn_ds_read_tr16_b64_v4i16((__attribute__((address_space(3))) v4i16_t*)p));
}
DEV u32x4 cat(u32x2 a, u32x2 b) { return u32x4{a.x, a.y, b.x, b.y}; }
DEV void dma16(const void* sbase, u32 voff, u32 lds) {
  u32 keep;
  asm volatile("s_mov_b32 %0, m0\n\ts_mov_b32 m0, %3\n\ts_nop 0\n\tglobal_load_lds_dwordx4 %1, %2\n\ts_mov_b32 m0, %0"
               : "=&s"(keep) : "v"(voff), "s"(sbase), "s"(lds) : "memory");
}
DEV void pf4(const void* sbase, u32 voff, u32 lds) {
  u32 keep;
  asm volatile("s_mov_b32 %0, m0\n\ts_mov_b32 m0, %3\n\ts_nop 0\n\tglobal_load_lds_dword %1, %2\n\ts_mov_b32 m0, %0"
               : "=&s"(keep) : "v"(voff), "s"(sbase), "s"(lds) : "memory");
}
#define BAR() asm volatile("s_waitcnt lgkmcnt(0)\n\ts_barrier" ::: "memory")
#define WAITVM(n) asm volatile("s_waitcnt vmcnt(" #n ")" ::: "memory")
DEV u32x4 ld_dev(rsrc_t rs, u32 off) { return __builtin_amdgcn_raw_buffer_load_b128(rs, off, 0, 16); }
DEV void st_dev(rsrc_t rs, u32 off, u32x4 v) { __builtin_amdgcn_raw_buffer_store_b128(v, rs, off, 0, 16); }
DEV void st_dev2(rsrc_t rs, u32 off, u32x2 v) { __builtin_amdgcn_raw_buffer_store_b64(v, rs, off, 0, 16); }
DEV u32 nosent(u32 v) { return v < 0xFFFFFFFEu ? v : 0xFFFFFFFEu; }
DEV bool has_sent(u32x4 v) { return __builtin_elementwise_max(__builtin_elementwise_max(v.x, v.y), __builtin_elementwise_max(v.z, v.w)) == SENT; }
DEV bool wave_any(bool p) { return __builtin_amdgcn_ballot_w64(p) != 0; }

typedef unsigned long long u64;
DEV u64 ballot(bool p) { return __builtin_amdgcn_ballot_w64(p); }
template <int N> DEV void poll_tagged(rsrc_t rs, const u32 (&off)[N], u32 tag, u32x4 (&v)[N]) {
  for (int spin = SPIN_MAX; spin > 0; --spin) {
    asm volatile("" ::: "memory");
#pragma unroll
    for (int i = 0; i < N; ++i) v[i] = ld_dev(rs, off[i]);
    u64 bad = 0;
#pragma unroll
    for (int i = 0; i < N; ++i) bad |= ballot(v[i].y != tag) | ballot(v[i].w != tag);
    if (bad == 0) break;
  }
}
DEV u32 umax(u32 a, u32 b) { return a > b ? a : b; }
DEV u32 umax4(u32x4 v) { return umax(umax(v.x, v.y), umax(v.z, v.w)); }
DEV u32x4 granule(f32x4 a, u32 tag) { return u32x4{cvtpk(a[0], a[1]), tag, cvtpk(a[2], a[3]), tag}; }

template <int NP> DEV f32x4 reduce_lines(rsrc_t rs, u32 base, u32 tag, u8* red, int t, int w, int l) {
  u32 off[2]; u32x4 v[2];
  const bool two = (t >> 3) + 64 < NP;
  off[0] = base + (t >> 3) * LS + (t & 7) * 16;
  off[1] = two ? off[0] + 64 * LS : off[0];
  poll_tagged<2>(rs, off, tag, v);
  const u32 x1 = two ? v[1].x : 0u, z1 = two ? v[1].z : 0u;
  float a0 = bflo(v[0].x) + bflo(x1), a1 = bfhi(v[0].x) + bfhi(x1);
  float a2 = bflo(v[0].z) + bflo(z1), a3 = bfhi(v[0].z) + bfhi(z1);
  float r0 = red32(a0, a2), r1 = red32(a1, a3);
  float r = red16(r0, r1);
  r += dppf<ROR8>(r);
  if ((l & 8) == 0) *(float*)(red + w * 128 + (4 * (l & 7) + (l >> 4)) * 4) = r;
  BAR();
  f32x4 s = {0.f, 0.f, 0.f, 0.f};
  if (w == 0) {
    f32x4 c[8];
#pragma unroll
    for (int i = 0; i < 8; ++i) c[i] = *(const f32x4*)(red + i * 128 + (l & 7) * 16);
    s = ((c[0] + c[1]) + (c[2] + c[3])) + ((c[4] + c[5]) + (c[6] + c[7]));
  }
  return s;
}

__global__ void __launch_bounds__(512, 2)
mla_ffn_decode_kernel(const u16* __restrict__ x, const u16* __restrict__ ckv, const u16* __restrict__ wq,
                      const u16* __restrict__ wuk, const u16* __restrict__ wuv, const u16* __restrict__ wo,
                      const u16* __restrict__ wg, const u16* __restrict__ wu, const u16* __restrict__ wd,
                      u16* __restrict__ out, u8* ws) {
  __shared__ __attribute__((aligned(16))) u8 smem[LDS_BYTES + 256];
  const u32 lds0 = (u32)(size_t)(__attribute__((address_space(3))) u8*)smem;
  const int b = blockIdx.x, xc = b & 7, r = b >> 3, idx = r & 15;
  const int t = threadIdx.x, l = t & 63, q = l >> 4, j = l & 15;
  const int w = __builtin_amdgcn_readfirstlane(t >> 6);
  const f32x4 zero4 = {0.f, 0.f, 0.f, 0.f};

  if (w == 7) {
    u32 pf_lo, pf_hi;
    asm volatile("s_getpc_b64 s[20:21]\n\ts_mov_b32 %0, s20\n\ts_mov_b32 %1, s21" : "=s"(pf_lo), "=s"(pf_hi) : : "s20", "s21");
    const u8* pc = (const u8*)(((unsigned long long)pf_hi << 32) | (pf_lo & ~127u));
    u32 keep;
    if (l < 5 && r * 5 + l < PF_LINES)
      asm volatile("s_mov_b32 %0, m0\n\ts_mov_b32 m0, %3\n\ts_nop 0\n\tglobal_load_lds_dword %1, %2\n\ts_mov_b32 m0, %0"
               : "=&s"(keep) : "v"((u32)((r * 5 + l) * 128)), "s"(pc), "s"(lds0 + LDS_BYTES) : "memory");
  }

  asm volatile("" : : "s"(x), "s"(ckv), "s"(wq), "s"(wuk), "s"(wuv), "s"(wo), "s"(wg), "s"(wu), "s"(wd), "s"(out), "s"(ws));
  const rsrc_t rs = __builtin_amdgcn_make_buffer_rsrc((void*)ws, 0, WS_END, 0x00020000);
  const u32 epoch_v = __builtin_amdgcn_raw_buffer_load_b32(rs, 0, 0, 16);

  if (r < 16) {
    const int h = 4 * xc + (idx >> 2), dh = (idx >> 1) & 1, ch = idx & 1;
    const u32 off2 = WS_R2 + (u32)((h * 2 + dh) * 8 + ch * 4 + (w >> 1)) * LS + (w & 1) * 64 + (4 * (j >> 3) + q) * 8;
    if (w == 0 && l < 32) st_dev(rs, WS_R2 + (u32)((h * 2 + dh) * 8 + ch * 4 + (l >> 3)) * LS + (l & 7) * 16, u32x4{SENT, SENT, SENT, SENT});
#pragma unroll
    for (int n = 0; n < 4; ++n) {
      const int row = 32 * w + 8 * n + (l >> 3);
      dma16(wuk, (u32)(((256 * ch + row) * 32 + h) * 256 + dh * 128 + (((l & 7) ^ ((row >> 1) & 7)) * 16)), lds0 + L_WUK + (32 * w + 8 * n) * 128);
    }
    const u32x4 xa = *(const u32x4*)((const u8*)x + l * 16);
    const u32 xb = *(const u32*)((const u8*)x + 1024 + l * 4);
    int kr[5];
#pragma unroll
    for (int k = 0; k < 5; ++k) { int v = k + 2 * ch; kr[k] = v >= 5 ? v - 5 : v; }
    u32 xw[5];
#pragma unroll
    for (int k = 0; k < 5; ++k) xw[k] = *(const u32*)((const u8*)x + (80 * w + 16 * kr[k] + 2 * (l >> 3)) * 2);
    u32x4 wv[10];
    {
      const rsrc_t rq = __builtin_amdgcn_make_buffer_rsrc((void*)((const u8*)wq + h * 256 + dh * 128), 0, 640 * 8192 - 8192 + 128, 0x00020000);
      const u32 vo = (u32)((80 * w + 2 * (l >> 3)) * 8192 + (l & 7) * 16);
#pragma unroll
      for (int i = 0; i < 10; ++i) wv[i] = __builtin_amdgcn_raw_buffer_load_b128(rq, vo, (16 * kr[i >> 1] + (i & 1)) * 8192, 0);
    }
    __builtin_amdgcn_sched_barrier(0);
    u32 xn[10];
    {
      float v[10] = {bflo(xa.x), bfhi(xa.x), bflo(xa.y), bfhi(xa.y), bflo(xa.z), bfhi(xa.z), bflo(xa.w), bfhi(xa.w), bflo(xb), bfhi(xb)};
      float s = 0.f;
#pragma unroll
      for (int i = 0; i < 10; ++i) s += v[i];
      const float mean = wave_sum(s) * (1.0f / 640.0f);
      float s2 = 0.f;
#pragma unroll
      for (int i = 0; i < 10; ++i) { float d = v[i] - mean; s2 += d * d; }
      const float rstd = 1.0f / sqrtf(wave_sum(s2) * (1.0f / 640.0f) + 1e-5f);
#pragma unroll
      for (int i = 0; i < 10; ++i) {
        const float xv = (((i & 1) ? bfhi(xw[i >> 1]) : bflo(xw[i >> 1])) - mean) * rstd;
        xn[i] = cvtpk(xv, xv);
      }
    }
    WAITVM(10);
    const u32 tag = __builtin_amdgcn_readfirstlane(epoch_v) + 1u;
    st_dev(rs, (t == 0) ? WS_PF + (u32)(h * LS + (dh * 2 + ch) * 16) : OOB, u32x4{tag, tag, tag, tag});
    u32x4 auk[2][2];
#pragma unroll
    for (int tt = 0; tt < 2; ++tt)
#pragma unroll
      for (int ks = 0; ks < 2; ++ks)
        auk[tt][ks] = *(const u32x4*)(smem + L_WUK + (32 * w + 16 * tt + j) * 128 + (((4 * ks + q) ^ (j >> 1)) * 16));
    {
      u32 msk[4];
#pragma unroll
      for (int k = 0; k < 4; ++k) msk[k] = (((j & 7) >> 1) == k) ? ((j & 1) ? 0xffff0000u : 0x0000ffffu) : 0u;
      f32x4 acc0 = zero4, acc1 = zero4;
#pragma unroll
      for (int i = 0; i < 10; ++i) {
        const u32x4 bf = {xn[i] & msk[0], xn[i] & msk[1], xn[i] & msk[2], xn[i] & msk[3]};
        if (i & 1) acc1 = mfma32(wv[i], bf, acc1); else acc0 = mfma32(wv[i], bf, acc0);
      }
      acc0 += acc1;
      if ((q >> 1) == (j >> 3)) {
        const u32 p01 = cvtpk(acc0[0], acc0[1]), p23 = cvtpk(acc0[2], acc0[3]);
        u8* dst = smem + L_QP + (2 * w + (j >> 3)) * 128 + (j & 7) * 2;
        const int c0 = 4 * (q & 1);
        *(u16*)(dst + (((c0 + 0) ^ w) * 16)) = (u16)p01;
        *(u16*)(dst + (((c0 + 1) ^ w) * 16)) = (u16)(p01 >> 16);
        *(u16*)(dst + (((c0 + 2) ^ w) * 16)) = (u16)p23;
        *(u16*)(dst + (((c0 + 3) ^ w) * 16)) = (u16)(p23 >> 16);
      }
    }
    BAR();
    {
      const u32x4 bq0 = *(const u32x4*)(smem + L_QP + j * 128 + ((q ^ (j >> 1)) * 16));
      const u32x4 bq1 = *(const u32x4*)(smem + L_QP + j * 128 + (((4 + q) ^ (j >> 1)) * 16));
      f32x4 a0 = mfma32(auk[0][0], bq0, zero4), a1 = mfma32(auk[1][0], bq0, zero4);
      a0 = mfma32(auk[0][1], bq1, a0); a1 = mfma32(auk[1][1], bq1, a1);
      f32x4 a;
#pragma unroll
      for (int e = 0; e < 4; ++e) {
        const float s0 = a0[e] + dppf<ROR8>(a0[e]), s1 = a1[e] + dppf<ROR8>(a1[e]);
        float v = (j < 8) ? s0 : s1;
        v += dppf<HMIR>(v); v += dppf<XOR2>(v); v += dppf<XOR1>(v);
        a[e] = v * (0.08838834764831845f * 1.4426950408889634f);
      }
      if ((j & 7) == 0) st_dev2(rs, off2, u32x2{nosent(cvtpk(a[0], a[1])), nosent(cvtpk(a[2], a[3]))});
    }
    BAR();
    const int jq = idx & 3, g = h >> 3;
#pragma unroll
    for (int n = 0; n < 4; ++n) {
      int row = 64 * w + 16 * n + (l >> 2);
      dma16(wuv, (u32)((row * 32 + h) * 256 + jq * 64 + (l & 3) * 16), lds0 + L_WUV + (64 * w + 16 * n) * 64);
    }
#pragma unroll
    for (int n = 0; n < 5; ++n)
      dma16(wo, (u32)((h * 128 + 32 * jq) * 1280 + (5 * w + n) * 1024 + l * 16), lds0 + L_WO + (5 * w + n) * 1024);
    if (w == 0) {
      u32 off[1] = {WS_FL + (u32)(g * 32 + (l & 31)) * LS}; u32x4 v[1];
      poll_tagged<1>(rs, off, tag, v);
    }
    WAITVM(0);
    BAR();
    u32x4 buv[2][2];
    {
      const int rr = j >> 2, p = j & 3;
      const int c0 = 64 * w + 16 * (q >> 1) + 2 * (q & 1) + 32 * (rr & 1) + 4 * (rr >> 1);
#pragma unroll
      for (int dt = 0; dt < 2; ++dt)
#pragma unroll
        for (int a = 0; a < 2; ++a) {
          const u8* p0 = smem + L_WUV + (c0 + a) * 64 + dt * 32 + p * 8;
          buv[dt][a] = cat(trrd(p0), trrd(p0 + 8 * 64));
        }
    }
    u32x4 awo[3][2];
    {
      const int rr = j >> 2, p = j & 3;
#pragma unroll
      for (int pi = 0; pi < 3; ++pi) {
        int PP = w + 8 * pi; PP = PP < 20 ? PP : w;
#pragma unroll
        for (int tt = 0; tt < 2; ++tt) {
          const u8* p0 = smem + L_WO + (8 * q + rr) * 1280 + (32 * PP + 16 * tt + 4 * p) * 2;
          awo[pi][tt] = cat(trrd(p0), trrd(p0 + 4 * 1280));
        }
      }
    }
    const int PP5 = w + 8 * (j >> 1);
    const u32 off5 = (j < 6 && PP5 < 20) ? WS_R5 + (u32)(PP5 * 128 + h * 4 + jq) * LS + (4 * (j & 1) + q) * 16 : OOB;
    {
      u32 off[1] = {WS_ML + (u32)(g * 32 + 4 * w + (l & 3)) * LS + (h & 7) * 16}; u32x4 v[1];
      poll_tagged<1>(rs, off, tag, v);
      int s = 4 * w + l;
      if (l < 4) *(u32x2*)(smem + L_MLB + (s & 7) * 32 + (s >> 3) * 8) = u32x2{v[0].x, v[0].z};
    }
    BAR();
    const u32 o3 = WS_R3 + (u32)((h * 32 + (l >> 3)) * 8 + w) * LS + (l & 7) * 16;
    u32x4 v3[4];
#pragma unroll
    for (int i = 0; i < 4; ++i) v3[i] = ld_dev(rs, o3 + i * 64 * LS);
    __builtin_amdgcn_sched_barrier(0);
    float e[4];
    u32 afm[2][4];
    {
      const f32x4 m01 = *(const f32x4*)(smem + L_MLB + (l >> 3) * 32), m23 = *(const f32x4*)(smem + L_MLB + (l >> 3) * 32 + 16);
      float mx = fmaxf(fmaxf(m01[0], m01[2]), fmaxf(m23[0], m23[2]));
      mx = fmaxf(mx, dppf<ROR8>(mx)); mx = max16(mx); mx = max32(mx);
      e[0] = __builtin_amdgcn_exp2f(m01[0] - mx); e[1] = __builtin_amdgcn_exp2f(m01[2] - mx);
      e[2] = __builtin_amdgcn_exp2f(m23[0] - mx); e[3] = __builtin_amdgcn_exp2f(m23[2] - mx);
      float L = e[0] * m01[1] + e[1] * m01[3] + e[2] * m23[1] + e[3] * m23[3];
      L += dppf<ROR8>(L); L = red16(L, L); L = red32(L, L);
      const float inv = __builtin_amdgcn_rcpf(L);
#pragma unroll
      for (int i = 0; i < 4; ++i) { e[i] *= inv; asm volatile("" : "+v"(e[i])); }
      const u32 hm = (l & 1) ? 0xffff0000u : 0x0000ffffu;
#pragma unroll
      for (int a = 0; a < 2; ++a)
#pragma unroll
        for (int k = 0; k < 4; ++k) { afm[a][k] = (((l & 7) >> 1) == k && ((l >> 3) & 1) == a) ? hm : 0u; asm volatile("" : "+v"(afm[a][k])); }
    }
    __builtin_amdgcn_sched_barrier(0);
    for (int spin = SPIN_MAX; spin > 0; --spin) {
      const u32x4 mx = __builtin_elementwise_max(__builtin_elementwise_max(v3[0], v3[1]), __builtin_elementwise_max(v3[2], v3[3]));
      if (ballot(has_sent(mx)) == 0) break;
      asm volatile("" ::: "memory");
#pragma unroll
      for (int i = 0; i < 4; ++i) v3[i] = ld_dev(rs, o3 + i * 64 * LS);
    }
    {
      float acc[8];
#pragma unroll
      for (int k = 0; k < 4; ++k) { acc[2 * k] = e[0] * bflo(v3[0][k]); acc[2 * k + 1] = e[0] * bfhi(v3[0][k]); }
#pragma unroll
      for (int i = 1; i < 4; ++i)
#pragma unroll
        for (int k = 0; k < 4; ++k) { acc[2 * k] += e[i] * bflo(v3[i][k]); acc[2 * k + 1] += e[i] * bfhi(v3[i][k]); }
      float r4[4], r2[2];
#pragma unroll
      for (int k = 0; k < 4; ++k) r4[k] = red32(acc[k], acc[k + 4]);
#pragma unroll
      for (int k = 0; k < 2; ++k) r2[k] = red16(r4[k], r4[k + 2]);
      const bool ah = (l & 8) != 0;
      float keep = ah ? r2[1] : r2[0], give = ah ? r2[0] : r2[1];
      float val = keep + dppf<ROR8>(give);
      const u32 pk = cvtpk(val, val);
      f32x4 d0 = zero4, d1 = zero4;
#pragma unroll
      for (int a = 0; a < 2; ++a) {
        u32x4 af;
#pragma unroll
        for (int k = 0; k < 4; ++k) af[k] = pk & afm[a][k];
        d0 = mfma32(af, buv[0][a], d0);
        d1 = mfma32(af, buv[1][a], d1);
      }
      float s0 = (d0[0] + d0[1]) + (d0[2] + d0[3]), s1 = (d1[0] + d1[1]) + (d1[2] + d1[3]);
      float s = red32(s0, s1);
      s = red16(s, s);
      if ((q & 1) == 0) *(float*)(smem + L_CTX + (16 * (l >> 5) + j) * 32 + w * 4) = s;
    }
    BAR();
    {
      const int d = 8 * q + (j & 7);
      const f32x4 c0 = *(const f32x4*)(smem + L_CTX + d * 32), c1 = *(const f32x4*)(smem + L_CTX + d * 32 + 16);
      const f32x4 cs = c0 + c1;
      const float v = (cs[0] + cs[1]) + (cs[2] + cs[3]);
      const u32 pk = cvtpk(v, dppf<XOR1>(v));
      const u32x4 bc = {dppu<NBC + 0>(pk), dppu<NBC + 2>(pk), dppu<NBC + 4>(pk), dppu<NBC + 6>(pk)};
      f32x4 a0 = zero4, a1 = zero4;
#pragma unroll
      for (int pi = 0; pi < 3; ++pi) {
        const u32x4 b0 = (j == 2 * pi) ? bc : u32x4{0, 0, 0, 0}, b1 = (j == 2 * pi + 1) ? bc : u32x4{0, 0, 0, 0};
        a0 = mfma32(awo[pi][0], b0, a0);
        a1 = mfma32(awo[pi][1], b1, a1);
      }
      st_dev(rs, off5, granule(a0 + a1, tag));
    }
  } else {
    const int g = idx & 3, s = 4 * xc + (idx >> 2);
    const u32 off3 = WS_R3 + (u32)(((8 * g + (j & 7)) * 32 + s) * 8 + w) * LS + 32 * q + 16 * (j >> 3);
    st_dev(rs, off3, u32x4{SENT, SENT, SENT, SENT});
    if (w == 6) {
      const u32 cq = (u32)(3 - ((size_t)wq >> 7)) & 7u, ck = (u32)(3 - ((size_t)wuk >> 7)) & 7u;
      if (l < 40) pf4(wq, (u32)((40 * idx + l) * 8192 + xc * 1024) + cq * 128, lds0 + LDS_BYTES);
      if (l < 32) pf4(wuk, (u32)((32 * idx + l) * 8192 + xc * 1024) + ck * 128, lds0 + LDS_BYTES);
    }
    {
      const u8* base = (const u8*)ckv + (size_t)(128 * s + 16 * w) * 1024;
#pragma unroll
      for (int n = 0; n < 16; ++n) {
        const int nr = (n + 4 * g) & 15;
        dma16(base + nr * 1024, (u32)(l * 16), lds0 + L_CKV + (16 * w + nr) * CKS);
      }
    }
    const int Q = xc + 8 * (idx >> 2);
    auto pf_s4w = [&](int v) {
      const int hh = 4 * xc + (idx >> 2);
      const int n = v * 64 + l;
      pf4(wuv, (u32)((32 * idx + (n >> 3)) * 8192 + xc * 1024 + (n & 7) * 128), lds0 + LDS_BYTES);
      pf4(wo, (u32)((hh * 128 + 32 * g) * 1280 + n * 128), lds0 + LDS_BYTES);
      if (v == 0) pf4(wo, (u32)((hh * 128 + 32 * g) * 1280 + (256 + l) * 128), lds0 + LDS_BYTES);
    };
    WAITVM(0);
    BAR();
    const u32 tag = __builtin_amdgcn_readfirstlane(epoch_v) + 1u;
    if (w == 0 && l < 8) st_dev(rs, WS_FL + (u32)(g * 32 + s) * LS + l * 16, u32x4{tag, tag, tag, tag});
    u32x4 aqk[16];
#pragma unroll
    for (int ks = 0; ks < 16; ++ks) aqk[ks] = *(const u32x4*)(smem + L_CKV + (16 * w + j) * CKS + ks * 64 + 16 * q);
    u32x4 apv[4][4];
    {
      const int rr = j >> 2, p = j & 3;
      const u8* p0 = smem + L_CKV + (4 * q + rr) * CKS + (64 * w + 4 * p) * 2;
#pragma unroll
      for (int kt = 0; kt < 4; ++kt)
#pragma unroll
        for (int tt = 0; tt < 4; ++tt)
          apv[kt][tt] = cat(trrd(p0 + (32 * kt) * CKS + tt * 32), trrd(p0 + (32 * kt + 16) * CKS + tt * 32));
    }
    if (w >= 4) pf_s4w(w - 4);
    if (w == 0) {
      const u32 of = WS_PF + (u32)(8 * g + ((l & 31) >> 2)) * LS + (l & 3) * 16;
      for (int spin = 0; spin < SPIN_MAX; ++spin) {
        asm volatile("" ::: "memory");
        const u32x4 f = ld_dev(rs, of);
        if (!wave_any((f.x != tag) | (f.y != tag) | (f.z != tag) | (f.w != tag))) break;
      }
    }
    BAR();
    if (w < 4) {
      u32 o2[4]; u32x4 p[4];
#pragma unroll
      for (int i = 0; i < 4; ++i) { o2[i] = WS_R2 + (u32)(((8 * g + 2 * w) * 2 + i) * 8 + (l >> 3)) * LS + (l & 7) * 16; p[i] = u32x4{SENT, SENT, SENT, SENT}; }
      for (int spin = 0; spin < SPIN_MAX; ++spin) {
        asm volatile("" ::: "memory");
        u32x4 nv[4];
#pragma unroll
        for (int i = 0; i < 4; ++i) nv[i] = ld_dev(rs, o2[i]);
        bool bad = false;
#pragma unroll
        for (int i = 0; i < 4; ++i) {
          const bool open = o2[i] != OOB, ok = !has_sent(nv[i]);
          if (open) p[i] = nv[i];
          o2[i] = (open & !ok) ? o2[i] : OOB;
          bad |= open & !ok;
        }
        if (!wave_any(bad)) break;
      }
#pragma unroll
      for (int a = 0; a < 2; ++a) {
        u32x4 qv;
#pragma unroll
        for (int k = 0; k < 4; ++k) qv[k] = cvtpk(bflo(p[2 * a][k]) + bflo(p[2 * a + 1][k]), bfhi(p[2 * a][k]) + bfhi(p[2 * a + 1][k]));
        *(u32x4*)(smem + L_QL + (2 * w + a) * CKS + l * 16) = qv;
      }
    }
    BAR();
    f32x4 sc;
    {
      f32x4 lo = zero4, hi = zero4;
      const u8* qb = smem + L_QL + (j & 7) * CKS + (j >> 3) * 512 + 16 * q;
      u32x4 bq[8];
#pragma unroll
      for (int u = 0; u < 8; ++u) bq[u] = *(const u32x4*)(qb + u * 64);
      __builtin_amdgcn_sched_barrier(0);
#pragma unroll
      for (int u = 0; u < 8; ++u) {
        lo = mfma32(aqk[u], bq[u], lo);
        hi = mfma32(aqk[u + 8], bq[u], hi);
      }
#pragma unroll
      for (int e = 0; e < 4; ++e) { float v = (j < 8) ? lo[e] : hi[e]; sc[e] = v + dppf<ROR8>(v); }
    }
    {
      float m = fmaxf(fmaxf(sc[0], sc[1]), fmaxf(sc[2], sc[3]));
      m = max16(m); m = max32(m);
      if (l < 8) *(float*)(smem + L_WM + l * 32 + w * 4) = m;
    }
    BAR();
    float mx;
    {
      const f32x4 a = *(const f32x4*)(smem + L_WM + (j & 7) * 32), c = *(const f32x4*)(smem + L_WM + (j & 7) * 32 + 16);
      mx = fmaxf(fmaxf(fmaxf(a[0], a[1]), fmaxf(a[2], a[3])), fmaxf(fmaxf(c[0], c[1]), fmaxf(c[2], c[3])));
      float p0 = __builtin_amdgcn_exp2f(sc[0] - mx), p1 = __builtin_amdgcn_exp2f(sc[1] - mx);
      float p2 = __builtin_amdgcn_exp2f(sc[2] - mx), p3 = __builtin_amdgcn_exp2f(sc[3] - mx);
      *(u32x2*)(smem + L_PF + (w >> 1) * 1024 + l * 16 + (w & 1) * 8) = u32x2{cvtpk(p0, p1), cvtpk(p2, p3)};
      float ls = (p0 + p1) + (p2 + p3);
      ls = red16(ls, ls); ls = red32(ls, ls);
      if (l < 8) *(float*)(smem + L_LP + l * 32 + w * 4) = ls;
    }
    BAR();
    {
      u32x4 pf[4];
#pragma unroll
      for (int kt = 0; kt < 4; ++kt) pf[kt] = *(const u32x4*)(smem + L_PF + kt * 1024 + l * 16);
      if (w == 0) {
        const f32x4 a = *(const f32x4*)(smem + L_LP + (j & 7) * 32), c = *(const f32x4*)(smem + L_LP + (j & 7) * 32 + 16);
        const f32x4 u = a + c;
        const float lt = (u[0] + u[1]) + (u[2] + u[3]);
        if (l < 8) st_dev(rs, WS_ML + (u32)(g * 32 + s) * LS + l * 16, u32x4{__float_as_uint(mx), tag, __float_as_uint(lt), tag});
      }
      f32x4 o[4] = {zero4, zero4, zero4, zero4};
#pragma unroll
      for (int kt = 0; kt < 4; ++kt)
#pragma unroll
        for (int tt = 0; tt < 4; ++tt) o[tt] = mfma32(apv[kt][tt], pf[kt], o[tt]);
      const bool hb = j >= 8;
      const f32x4 oa = hb ? o[2] : o[0], ob = hb ? o[3] : o[1];
      u32x4 pv = {cvtpk(oa[0], oa[1]), cvtpk(oa[2], oa[3]), cvtpk(ob[0], ob[1]), cvtpk(ob[2], ob[3])};
#pragma unroll
      for (int k = 0; k < 4; ++k) pv[k] = pv[k] < 0xFFFFFFFEu ? pv[k] : 0xFFFFFFFEu;
      st_dev(rs, off3, pv);
    }
    if (Q < 27) {
      const int fg = 4 * Q + (idx & 3);
#pragma unroll
      for (int n = 0; n < 3; ++n) {
        const int i0 = w + 8 * n;
        if (i0 < 20) {
          const int ir = i0 + 5 * (idx & 3), it = ir >= 20 ? ir - 20 : ir;
          const u32 vo = (u32)((32 * it + (l >> 1)) * 3456 + fg * 32 + (l & 1) * 16);
          dma16(wg, vo, lds0 + L_GATE + it * 1024);
          dma16(wu, vo, lds0 + L_UP + it * 1024);
          dma16(wd, (u32)(fg * 16 * 1280 + it * 1024 + l * 16), lds0 + L_DOWN + it * 1024);
        }
      }
      if (t < 320) *(u32*)(smem + L_ONES + t * 4) = 0x3F803F80u;
      WAITVM(0);
      BAR();
      const int rr = j >> 2, p = j & 3;
      u32x4 agu[2][5];
      if (w < 4) {
#pragma unroll
        for (int mat = 0; mat < 2; ++mat)
#pragma unroll
          for (int kk = 0; kk < 5; ++kk) {
            const u8* p0 = smem + (mat ? L_UP : L_GATE) + (32 * (5 * w + kk) + 8 * q + rr) * 32 + p * 8;
            agu[mat][kk] = cat(trrd(p0), trrd(p0 + 4 * 32));
          }
      }
      u32x2 adn[3][2];
#pragma unroll
      for (int pi = 0; pi < 3; ++pi) {
        int PP = w + 8 * pi; PP = PP < 20 ? PP : w;
#pragma unroll
        for (int tt = 0; tt < 2; ++tt) adn[pi][tt] = trrd(smem + L_DOWN + (4 * q + rr) * 1280 + (32 * PP + 16 * tt + 4 * p) * 2);
      }
      const int PP7 = w + 8 * (j >> 1);
      const u32 off7 = (j < 6 && PP7 < 20) ? WS_R7 + (u32)(PP7 * 108 + fg) * LS + (4 * (j & 1) + q) * 16 : OOB;
      if (w < 3) {
        const int t6 = t < 160 ? t : t - 32;
        u32 off[1] = {WS_R6 + (u32)((idx & 3) * 32 + (t6 >> 3)) * LS + (t6 & 7) * 16}; u32x4 v[1];
        poll_tagged<1>(rs, off, tag, v);
        if (t < 160) *(u32x2*)(smem + L_HB + t * 8) = u32x2{v[0].x, v[0].z};
      }
      BAR();
      if (w < 4) {
        f32x4 ga = zero4, ua = zero4;
        const u8* hbp = smem + ((j == 1) ? L_ONES : L_HB) + (160 * w + 8 * q) * 2;
        u32x4 bh[5];
#pragma unroll
        for (int kk = 0; kk < 5; ++kk) bh[kk] = *(const u32x4*)(hbp + kk * 64);
        __builtin_amdgcn_sched_barrier(0);
#pragma unroll
        for (int kk = 0; kk < 5; ++kk) {
          ga = mfma32(agu[0][kk], bh[kk], ga);
          ua = mfma32(agu[1][kk], bh[kk], ua);
        }
        if (j < 2) {
#pragma unroll
          for (int e = 0; e < 4; ++e) {
            *(float*)(smem + L_GP + ((0 + j) * 16 + 4 * q + e) * 16 + w * 4) = ga[e];
            *(float*)(smem + L_GP + ((2 + j) * 16 + 4 * q + e) * 16 + w * 4) = ua[e];
          }
        }
      } else if (w == 4) {
        const u32x4 ha = *(const u32x4*)(smem + L_HB + l * 16);
        const u32 hc = *(const u32*)(smem + L_HB + 1024 + l * 4);
        const float K = bflo(__builtin_amdgcn_readfirstlane(ha.x));
        const float d[10] = {bflo(ha.x) - K, bfhi(ha.x) - K, bflo(ha.y) - K, bfhi(ha.y) - K, bflo(ha.z) - K,
                             bfhi(ha.z) - K, bflo(ha.w) - K, bfhi(ha.w) - K, bflo(hc) - K, bfhi(hc) - K};
        float s1 = (((d[0] + d[1]) + (d[2] + d[3])) + ((d[4] + d[5]) + (d[6] + d[7]))) + (d[8] + d[9]);
        float s2 = (((d[0] * d[0] + d[1] * d[1]) + (d[2] * d[2] + d[3] * d[3])) + ((d[4] * d[4] + d[5] * d[5]) + (d[6] * d[6] + d[7] * d[7]))) + (d[8] * d[8] + d[9] * d[9]);
        s1 = wave_sum(s1) * (1.0f / 640.0f); s2 = wave_sum(s2) * (1.0f / 640.0f);
        const float rstd = __builtin_amdgcn_rsqf(fmaxf(s2 - s1 * s1, 0.f) + 1e-5f);
        if (l == 0) *(f32x2*)(smem + L_ST) = f32x2{K + s1, rstd};
      }
      BAR();
      {
        const int mat = (j >> 2) & 1, f = 4 * q + (j & 3);
        const f32x4 hw = *(const f32x4*)(smem + L_GP + ((2 * mat + 0) * 16 + f) * 16);
        const f32x4 cs = *(const f32x4*)(smem + L_GP + ((2 * mat + 1) * 16 + f) * 16);
        const f32x2 st = *(const f32x2*)(smem + L_ST);
        __builtin_amdgcn_sched_barrier(0);
        const float val = st[1] * (((hw[0] + hw[1]) + (hw[2] + hw[3])) - st[0] * ((cs[0] + cs[1]) + (cs[2] + cs[3])));
        const float up = dppf<SHL4>(val);
        const float hh = (val > 0.f ? val : 0.f) * up;
        const u32 pk = cvtpk(hh, dppf<XOR1>(hh));
        const u32x2 bd = {dppu<NBC + 0>(pk), dppu<NBC + 2>(pk)};
        f32x4 a0 = zero4, a1 = zero4;
#pragma unroll
        for (int pi = 0; pi < 3; ++pi) {
          const u32x2 b0 = (j == 2 * pi) ? bd : u32x2{0, 0}, b1 = (j == 2 * pi + 1) ? bd : u32x2{0, 0};
          a0 = mfma16(adn[pi][0], b0, a0);
          a1 = mfma16(adn[pi][1], b1, a1);
        }
        st_dev(rs, off7, granule(a0 + a1, tag));
      }
    } else {
      const int cg = 4 * (Q - 27) + (idx & 3);
      const rsrc_t ro = __builtin_amdgcn_make_buffer_rsrc((void*)out, 0, 1280, 0x00020000);
      const u32x2 xv = *(const u32x2*)((const u8*)x + (32 * cg + 4 * (l & 7)) * 2);
      f32x4 hsum = reduce_lines<128>(rs, WS_R5 + (u32)cg * 128 * LS, tag, smem + L_RED5, t, w, l);
      f32x4 hv = {bflo(xv.x) + hsum[0], bfhi(xv.x) + hsum[1], bflo(xv.y) + hsum[2], bfhi(xv.y) + hsum[3]};
      if (w == 0 && l < 32) st_dev(rs, WS_R6 + (u32)((l >> 3) * 32 + cg) * LS + (l & 7) * 16, granule(hv, tag));
      f32x4 fsum = reduce_lines<108>(rs, WS_R7 + (u32)cg * 108 * LS, tag, smem + L_RED7, t, w, l);
      if (w == 0 && l < 8) {
        hv += fsum;
        __builtin_amdgcn_raw_buffer_store_b64(u32x2{cvtpk(hv[0], hv[1]), cvtpk(hv[2], hv[3])}, ro, (32 * cg + 4 * l) * 2, 0, 16);
        if (cg == 0 && l == 0) __builtin_amdgcn_raw_buffer_store_b32(tag, rs, 0, 0, 16);
      }
    }
  }
  WAITVM(0);
}

extern "C" void kernel_launch(void* const* d_in, const int* in_sizes, int n_in, void* d_out, int out_size, void* d_ws,
                              size_t ws_size, hipStream_t stream) {
  (void)in_sizes; (void)n_in; (void)out_size; (void)ws_size;
  mla_ffn_decode_kernel<<<256, 512, 0, stream>>>((const u16*)d_in[0], (const u16*)d_in[1], (const u16*)d_in[2],
                                                 (const u16*)d_in[3], (const u16*)d_in[4], (const u16*)d_in[5],
                                                 (const u16*)d_in[7], (const u16*)d_in[8], (const u16*)d_in[9],
                                                 (u16*)d_out, (u8*)d_ws);
}
```
